```python
import torch

def _softmax(x, axis=-1):
    return torch.softmax(x, dim=axis)
def _gelu(x):
    return torch.nn.functional.gelu(x, approximate="tanh")
def _silu(x):
    return torch.nn.functional.silu(x)
def _sigmoid(x):
    return torch.sigmoid(x)
def _layernorm(x, axis=-1, eps=1e-5):
    m = x.mean(dim=axis, keepdim=True)
    v = x.var(dim=axis, keepdim=True, unbiased=False)
    return (x - m) / torch.sqrt(v + eps)
def _scatter_add(tgt, idx, src, axis=0):
    return tgt.clone().index_add_(axis, idx, src)
def _segment_sum(src, seg, n):
    out = torch.zeros((n,) + src.shape[1:], dtype=src.dtype)
    return out.index_add_(0, seg, src)
def _pad(x, pw):
    flat = tuple(v for pair in reversed(pw) for v in pair)
    return torch.nn.functional.pad(x, flat)

kernel_name = 'parallel_sum__sliding_block_cached__mixer_token_mlp__decode'

def setup_inputs():

    _w = (lambda a: (lambda t: t.to(torch.bfloat16) if t.is_floating_point() else t)(torch.as_tensor(a)))
    def _A_setup():
        B, Sq, D = 1, 1, 448
        Hq, Hkv, Sk, Dh = 16, 8, 32768, 256
        assert Hq % Hkv == 0, (Hq, Hkv)
        return {
            "x": _w((lambda *s: torch.randn(*s, dtype=torch.bfloat16) * 0.05)(B, Sq, D)),
            "k_cache": _w((lambda *s: torch.randn(*s, dtype=torch.bfloat16) * 1.6)(B, Hkv, Sk, Dh)),
            "v_cache": _w((lambda *s: torch.randn(*s, dtype=torch.bfloat16) * 0.8)(B, Hkv, Sk, Dh)),
            "wq": _w((lambda *s: torch.randn(*s, dtype=torch.bfloat16) * 1.6)(D, Hq * Dh) * (D ** -0.5)),
            "wo": _w((lambda *s: torch.randn(*s, dtype=torch.bfloat16) * 0.8)(Hq * Dh, D) * ((Hq * Dh) ** -0.5)),
        }
    _A_in = _A_setup()

    def _B_setup():
        B, S, D, F = 1, 1, 448, 1792
        return {
            "x": _w((lambda *s: torch.randn(*s, dtype=torch.bfloat16) * 0.05)(B, S, D)),
            "w1": _w((lambda *s: torch.randn(*s, dtype=torch.bfloat16) * 0.4)(S, F) * (S ** -0.5)),
            "b1": _w((lambda *s: torch.randn(*s, dtype=torch.bfloat16) * 0.4)(F) * 0.02),
            "w2": _w((lambda *s: torch.randn(*s, dtype=torch.bfloat16) * 0.4)(F, S) * (F ** -0.5)),
            "b2": _w((lambda *s: torch.randn(*s, dtype=torch.bfloat16) * 0.4)(S) * 0.02),
        }
    _B_in = _B_setup()

    return {
        "A_x": _A_in["x"],
        "A_k_cache": _A_in["k_cache"],
        "A_v_cache": _A_in["v_cache"],
        "A_wq": _A_in["wq"],
        "A_wo": _A_in["wo"],
        "B_x": _B_in["x"],
        "B_w1": _B_in["w1"],
        "B_b1": _B_in["b1"],
        "B_w2": _B_in["w2"],
        "B_b2": _B_in["b2"],
    }

def reference(A_x, A_k_cache, A_v_cache, A_wq, A_wo, B_x, B_w1, B_b1, B_w2, B_b2):
    def _A_ref(x, k_cache, v_cache, wq, wo):
        B, Sq, D = x.shape
        Hq, Dh = 16, 256
        Hkv, Sk = k_cache.shape[1], k_cache.shape[2]
        W = min(4096, max(256, Sk // 4))
        rep = Hq // Hkv
        q = (lambda x, *d: x.permute(*d))((x @ wq).reshape(B, Sq, Hq, Dh), 0, 2, 1, 3)
        k = (lambda x, n, axis: torch.repeat_interleave(x, n, dim=axis))(k_cache[:, :, Sk - W:, :], rep, axis=1)
        v = (lambda x, n, axis: torch.repeat_interleave(x, n, dim=axis))(v_cache[:, :, Sk - W:, :], rep, axis=1)
        scores = torch.einsum("bhqd,bhkd->bhqk", q, k) * (Dh ** -0.5)
        p = _softmax(scores, axis=-1)
        o = torch.einsum("bhqk,bhkd->bhqd", p, v)
        return (lambda x, *d: x.permute(*d))(o, 0, 2, 1, 3).reshape(B, Sq, Hq * Dh) @ wo

    def _B_ref(x, w1, b1, w2, b2):
        B, S, D = x.shape
        h = _layernorm(x, axis=-1)
        u = torch.einsum("bsd,sf->bdf", h, w1) + b1
        u = _gelu(u)
        y = torch.einsum("bdf,fs->bds", u, w2) + b2
        return (lambda x, *d: x.permute(*d))(y, 0, 2, 1)

    x = A_x
    xn = _layernorm(x, axis=-1)
    return x + _A_ref(xn, A_k_cache, A_v_cache, A_wq, A_wo) + _B_ref(xn, B_w1, B_b1, B_w2, B_b2)
```

```cpp
#include <hip/hip_runtime.h>
#include <hip/hip_bf16.h>
#include <stdint.h>

typedef unsigned u32x4 __attribute__((ext_vector_type(4)));
typedef float f32x4 __attribute__((ext_vector_type(4)));
typedef float f32x2 __attribute__((ext_vector_type(2)));
typedef __bf16 bf16x2_t __attribute__((ext_vector_type(2)));

constexpr int D = 448, SK = 32768, WIN = 4096;
constexpr int WS_EPOCH = 0;
constexpr int WS_Q = 4096;
constexpr int WS_ML = WS_Q + 8 * 4 * 256 * 16;
constexpr int WS_O = WS_ML + 16 * 32 * 16;
constexpr int WS_P = WS_O + 16 * 16 * 256 * 16;
constexpr int WS_END = WS_P + 56 * 256 * 64;
constexpr unsigned SPIN_CAP = 20000;

struct Args {
  const __hip_bfloat16* x; const __hip_bfloat16* kc; const __hip_bfloat16* vc; const __hip_bfloat16* wq;
  const __hip_bfloat16* wo; const __hip_bfloat16* w1; const __hip_bfloat16* b1; const __hip_bfloat16* w2;
  const __hip_bfloat16* b2; __hip_bfloat16* out; unsigned char* ws;
};

__device__ __forceinline__ float asf(unsigned u) { return __builtin_bit_cast(float, u); }
__device__ __forceinline__ unsigned asu(float f) { return __builtin_bit_cast(unsigned, f); }
__device__ __forceinline__ float blo(unsigned u) { return asf(u << 16); }
__device__ __forceinline__ float bhi(unsigned u) { return asf(u & 0xffff0000u); }
__device__ __forceinline__ unsigned cvtpk(float lo, float hi) {
  unsigned r; asm("v_cvt_pk_bf16_f32 %0, %1, %2" : "=v"(r) : "v"(lo), "v"(hi)); return r;
}
__device__ __forceinline__ float bfr(float x) { return blo(cvtpk(x, x)); }
__device__ __forceinline__ float dot2(unsigned a, unsigned b, float acc) {
  return __builtin_amdgcn_fdot2_f32_bf16(__builtin_bit_cast(bf16x2_t, a), __builtin_bit_cast(bf16x2_t, b), acc, false);
}
template <int CTRL> __device__ __forceinline__ float dpp(float x) {
  return __builtin_bit_cast(float, __builtin_amdgcn_mov_dpp(__builtin_bit_cast(int, x), CTRL, 0xf, 0xf, true));
}
constexpr int XOR1 = 0xB1, XOR2 = 0x4E, HMIR = 0x141, RMIR = 0x140, ROR8 = 0x128, ROR4 = 0x124;
__device__ __forceinline__ float swap16_add(float a, float b) {
  auto s = __builtin_amdgcn_permlane16_swap(asu(a), asu(b), false, false);
  return asf(s[0]) + asf(s[1]);
}
__device__ __forceinline__ float swap32_add(float a, float b) {
  auto s = __builtin_amdgcn_permlane32_swap(asu(a), asu(b), false, false);
  return asf(s[0]) + asf(s[1]);
}
__device__ __forceinline__ float swap16_max(float a) {
  auto s = __builtin_amdgcn_permlane16_swap(asu(a), asu(a), false, false);
  return fmaxf(asf(s[0]), asf(s[1]));
}
__device__ __forceinline__ float swap32_max(float a) {
  auto s = __builtin_amdgcn_permlane32_swap(asu(a), asu(a), false, false);
  return fmaxf(asf(s[0]), asf(s[1]));
}
__device__ __forceinline__ float wave_sum(float v) {
  v += dpp<XOR1>(v); v += dpp<XOR2>(v); v += dpp<HMIR>(v); v += dpp<RMIR>(v);
  v = swap16_add(v, v); v = swap32_add(v, v); return v;
}
__device__ __forceinline__ float half_max(float v) {
  v = fmaxf(v, dpp<XOR1>(v)); v = fmaxf(v, dpp<XOR2>(v)); v = fmaxf(v, dpp<HMIR>(v)); v = fmaxf(v, dpp<RMIR>(v));
  return swap16_max(v);
}
__device__ __forceinline__ float half_sum(float v) {
  v += dpp<XOR1>(v); v += dpp<XOR2>(v); v += dpp<HMIR>(v); v += dpp<RMIR>(v);
  return swap16_add(v, v);
}
__device__ __forceinline__ u32x4 ld_sc1(__amdgpu_buffer_rsrc_t r, int off) { return __builtin_amdgcn_raw_buffer_load_b128(r, off, 0, 16); }
__device__ __forceinline__ void st_sc1(__amdgpu_buffer_rsrc_t r, int off, u32x4 v) { __builtin_amdgcn_raw_buffer_store_b128(v, r, off, 0, 16); }
__device__ __forceinline__ u32x4 ldnt(const void* p) { return __builtin_nontemporal_load(reinterpret_cast<const u32x4*>(p)); }
#define WG_BARRIER() asm volatile("s_waitcnt lgkmcnt(0)\n\ts_barrier" ::: "memory")

constexpr int L_XS = 0;
constexpr int L_Q = 448;
constexpr int L_TAG = 704;
constexpr int L_MAX = 708;
constexpr int L_L = 724;
constexpr int L_P = 740;
constexpr int L_O = 1024;
constexpr int L_END = L_O + 8 * 512;

__device__ __forceinline__ void layernorm8(const float (&v)[8], bool live, float (&o)[8]) {
  float s = 0.f;
#pragma unroll
  for (int e = 0; e < 8; ++e) s += v[e];
  s = live ? s : 0.f;
  const float mean = wave_sum(s) * (1.f / 448.f);
  float q = 0.f;
#pragma unroll
  for (int e = 0; e < 8; ++e) { const float d = v[e] - mean; q = fmaf(d, d, q); }
  q = live ? q : 0.f;
  const float var = bfr(wave_sum(q) * (1.f / 448.f));
  const float mb = bfr(mean);
  const float den = bfr(sqrtf(bfr(var + 1e-5f)));
#pragma unroll
  for (int e = 0; e < 8; ++e) o[e] = bfr(bfr(v[e] - mb) / den);
}

__global__ void __launch_bounds__(576, 3) fused_decode(Args a) {
  __shared__ __attribute__((aligned(16))) float lds[L_END];
  const int b = blockIdx.x, g = b & 7, s = b >> 3;
  const int lane = threadIdx.x & 63;
  const int wave = __builtin_amdgcn_readfirstlane(threadIdx.x >> 6);
  const __amdgpu_buffer_rsrc_t rs = __builtin_amdgcn_make_buffer_rsrc(a.ws, 0, WS_END, 0x00020000);

  if (wave < 8) {
    const int half = lane >> 5, c = lane & 31;
    const size_t base = ((size_t)g * SK + (SK - WIN) + s * 128 + wave * 16) * 256 + lane * 8;
    u32x4 K[8], V[8];
#pragma unroll
    for (int i = 0; i < 8; ++i) K[i] = ldnt(a.kc + base + i * 512);
#pragma unroll
    for (int i = 0; i < 8; ++i) V[i] = ldnt(a.vc + base + i * 512);
    WG_BARRIER();
    const u32x4 q0 = *reinterpret_cast<const u32x4*>(&lds[L_Q + 4 * c]);
    const u32x4 q1 = *reinterpret_cast<const u32x4*>(&lds[L_Q + 128 + 4 * c]);
    const unsigned tagA = asu(lds[L_TAG]), tagB = asu(lds[L_TAG + 1]);
    float x[16];
#pragma unroll
    for (int i = 0; i < 8; ++i) {
      float d0 = 0.f, d1 = 0.f;
#pragma unroll
      for (int k = 0; k < 4; ++k) { d0 = dot2(K[i][k], q0[k], d0); d1 = dot2(K[i][k], q1[k], d1); }
      x[2 * i] = d0; x[2 * i + 1] = d1;
    }
#define TR_STEP(N, CTRL, BIT) _Pragma("unroll") for (int h = 0; h < (N); ++h) { \
      const float keep_ = (c & (BIT)) ? x[h + (N)] : x[h], send_ = (c & (BIT)) ? x[h] : x[h + (N)]; \
      x[h] = keep_ + dpp<CTRL>(send_); }
    TR_STEP(8, ROR8, 8) TR_STEP(4, HMIR, 4) TR_STEP(2, XOR2, 2) TR_STEP(1, XOR1, 1)
#undef TR_STEP
    float sc = swap16_add(x[0], x[0]);
    sc = bfr(sc) * (0.0625f * 1.4426950408889634f);
    float wm = sc;
    wm = fmaxf(wm, dpp<ROR4>(wm)); wm = fmaxf(wm, dpp<ROR8>(wm)); wm = fmaxf(wm, dpp<XOR2>(wm)); wm = swap32_max(wm);
    if (lane < 2) lds[L_MAX + wave * 2 + lane] = wm;
    WG_BARRIER();
    float M = lds[L_MAX + (c & 1)];
#pragma unroll
    for (int w = 1; w < 8; ++w) M = fmaxf(M, lds[L_MAX + w * 2 + (c & 1)]);
    const float p = __builtin_amdgcn_exp2f(sc - M);
    float ls = p;
    ls += dpp<ROR4>(ls); ls += dpp<ROR8>(ls); ls += dpp<XOR2>(ls); ls = swap32_add(ls, ls);
    if (lane < 2) lds[L_L + wave * 2 + lane] = ls;
    float* prow = &lds[L_P + (wave * 2 + half) * 16];
    if (c < 16) prow[c] = p;
    f32x4 pr[4];
#pragma unroll
    for (int u = 0; u < 4; ++u) pr[u] = *reinterpret_cast<const f32x4*>(prow + 4 * u);
    float acc[16];
#pragma unroll
    for (int k = 0; k < 16; ++k) acc[k] = 0.f;
#pragma unroll
    for (int i = 0; i < 8; ++i) {
      const float p0 = pr[i >> 1][(2 * i) & 3], p1 = pr[i >> 1][(2 * i + 1) & 3];
#pragma unroll
      for (int k = 0; k < 4; ++k) {
        const float lo = blo(V[i][k]), hi = bhi(V[i][k]);
        acc[2 * k] = fmaf(p0, lo, acc[2 * k]); acc[2 * k + 1] = fmaf(p0, hi, acc[2 * k + 1]);
        acc[8 + 2 * k] = fmaf(p1, lo, acc[8 + 2 * k]); acc[8 + 2 * k + 1] = fmaf(p1, hi, acc[8 + 2 * k + 1]);
      }
    }
    float r[8];
#pragma unroll
    for (int k = 0; k < 8; ++k) r[k] = swap32_add(acc[k], acc[8 + k]);
    float* orow = &lds[L_O + wave * 512 + half * 256 + 8 * c];
    *reinterpret_cast<f32x4*>(orow) = f32x4{r[0], r[1], r[2], r[3]};
    *reinterpret_cast<f32x4*>(orow + 4) = f32x4{r[4], r[5], r[6], r[7]};
    WG_BARRIER();
    if (wave < 4) {
      const int t = wave * 64 + lane, hd = t >> 7, pi = t & 127;
      f32x2 o = *reinterpret_cast<const f32x2*>(&lds[L_O + 2 * t]);
#pragma unroll
      for (int w = 1; w < 8; ++w) { const f32x2 u = *reinterpret_cast<const f32x2*>(&lds[L_O + w * 512 + 2 * t]); o[0] += u[0]; o[1] += u[1]; }
      st_sc1(rs, WS_O + ((((2 * g + hd) * 16 + (pi >> 3)) * 32 + s) * 8 + (pi & 7)) * 16, u32x4{asu(o[0]), asu(o[1]), tagA, tagB});
      if (wave == 0 && lane < 2) {
        float L = 0.f;
#pragma unroll
        for (int w = 0; w < 8; ++w) L += lds[L_L + w * 2 + lane];
        st_sc1(rs, WS_ML + ((2 * g + lane) * 32 + s) * 16, u32x4{asu(M), asu(L), tagA, tagB});
      }
    }
    return;
  }

  const bool live = lane < 56;
  const int lc = live ? lane : 0;
  const u32x4 ep = ld_sc1(rs, WS_EPOCH);
  const u32x4 xraw = *reinterpret_cast<const u32x4*>(a.x + 8 * lc);
  const int kq = s >> 3, cb = s & 7;
  u32x4 wqv[14];
  {
    const __hip_bfloat16* wp = a.wq + (size_t)(kq * 112 + (lane >> 3)) * 4096 + g * 512 + cb * 64 + 8 * (lane & 7);
#pragma unroll
    for (int i = 0; i < 14; ++i) wqv[i] = ldnt(wp + (size_t)i * 8 * 4096);
  }
  float xv[8], xn[8], hh[8];
#pragma unroll
  for (int k = 0; k < 4; ++k) { xv[2 * k] = blo(xraw[k]); xv[2 * k + 1] = bhi(xraw[k]); }
  layernorm8(xv, live, xn);
  layernorm8(xn, live, hh);
  if (live) {
    *reinterpret_cast<f32x4*>(&lds[L_XS + 8 * lane]) = f32x4{xn[0], xn[1], xn[2], xn[3]};
    *reinterpret_cast<f32x4*>(&lds[L_XS + 8 * lane + 4]) = f32x4{xn[4], xn[5], xn[6], xn[7]};
  }
  const unsigned tagA = ep[0] + 1u, tagB = ep[1] + 0x9E3779B9u;
  {
    float qa[8];
#pragma unroll
    for (int e = 0; e < 8; ++e) qa[e] = 0.f;
#pragma unroll
    for (int i = 0; i < 14; ++i) {
      const float xs = lds[L_XS + kq * 112 + 8 * i + (lane >> 3)];
#pragma unroll
      for (int k = 0; k < 4; ++k) { qa[2 * k] = fmaf(xs, blo(wqv[i][k]), qa[2 * k]); qa[2 * k + 1] = fmaf(xs, bhi(wqv[i][k]), qa[2 * k + 1]); }
    }
#pragma unroll
    for (int h = 0; h < 4; ++h) {
      const float keep = (lane & 8) ? qa[h + 4] : qa[h], send = (lane & 8) ? qa[h] : qa[h + 4];
      qa[h] = keep + dpp<ROR8>(send);
    }
    qa[0] = swap16_add(qa[0], qa[2]); qa[1] = swap16_add(qa[1], qa[3]);
    qa[0] = swap32_add(qa[0], qa[0]); qa[1] = swap32_add(qa[1], qa[1]);
    if (lane < 32) {
      const int pidx = cb * 32 + 4 * (lane & 7) + 2 * ((lane >> 3) & 1) + ((lane >> 4) & 1);
      st_sc1(rs, WS_Q + ((g * 4 + kq) * 256 + pidx) * 16, u32x4{asu(qa[0]), asu(qa[1]), tagA, tagB});
    }
  }
  float po[8];
#pragma unroll
  for (int e = 0; e < 8; ++e) po[e] = 0.f;
#pragma unroll
  for (int j = 0; j < 7; ++j) {
    const int f = 7 * b + j;
    const float w1f = __bfloat162float(a.w1[f]), b1f = __bfloat162float(a.b1[f]), w2f = __bfloat162float(a.w2[f]);
#pragma unroll
    for (int e = 0; e < 8; ++e) {
      const float u = fmaf(hh[e], w1f, b1f);
      const float z = u * (1.f + 0.044715f * u * u) * (-2.f * 0.7978845608028654f * 1.4426950408889634f);
      const float gl = u * __builtin_amdgcn_rcpf(1.f + __builtin_amdgcn_exp2f(z));
      po[e] = fmaf(gl, w2f, po[e]);
    }
  }
  for (unsigned spin = 0;; ++spin) {
    u32x4 v[16];
#pragma unroll
    for (int j = 0; j < 4; ++j)
#pragma unroll
      for (int k = 0; k < 4; ++k) v[j * 4 + k] = ld_sc1(rs, WS_Q + ((g * 4 + k) * 256 + lane + 64 * j) * 16);
    bool ok = true;
#pragma unroll
    for (int i = 0; i < 16; ++i) ok = ok && (v[i][2] == tagA) && (v[i][3] == tagB);
    if (__all(ok) || spin > SPIN_CAP) {
#pragma unroll
      for (int j = 0; j < 4; ++j) {
        const float s0 = (asf(v[j * 4][0]) + asf(v[j * 4 + 1][0])) + (asf(v[j * 4 + 2][0]) + asf(v[j * 4 + 3][0]));
        const float s1 = (asf(v[j * 4][1]) + asf(v[j * 4 + 1][1])) + (asf(v[j * 4 + 2][1]) + asf(v[j * 4 + 3][1]));
        lds[L_Q + lane + 64 * j] = asf(cvtpk(s0, s1));
      }
      break;
    }
    __builtin_amdgcn_s_sleep(2);
  }
  if (lane == 0) { lds[L_TAG] = asf(tagA); lds[L_TAG + 1] = asf(tagB); }
  WG_BARRIER();
  const int h = 2 * g + (s >> 4), db = s & 15;
  u32x4 wov[16];
  {
    const __hip_bfloat16* wp = a.wo + (size_t)(h * 256 + db * 16) * D + 8 * lc;
#pragma unroll
    for (int r = 0; r < 16; ++r) wov[r] = ldnt(wp + r * D);
  }
  unsigned xo = 0; float b2f = 0.f;
  if (b < 56) { xo = *reinterpret_cast<const unsigned*>(a.x + 8 * b + 2 * (lane & 3)); b2f = __bfloat162float(a.b2[0]); }
  WG_BARRIER();
  WG_BARRIER();
  unsigned wpk[8][8];
#pragma unroll
  for (int p = 0; p < 8; ++p)
#pragma unroll
    for (int k = 0; k < 4; ++k) {
      wpk[p][2 * k] = __builtin_amdgcn_perm(wov[2 * p + 1][k], wov[2 * p][k], 0x05040100u);
      wpk[p][2 * k + 1] = __builtin_amdgcn_perm(wov[2 * p + 1][k], wov[2 * p][k], 0x07060302u);
    }
  unsigned opk = 0;
  for (unsigned spin = 0;; ++spin) {
    u32x4 v[4];
#pragma unroll
    for (int k = 0; k < 4; ++k) v[k] = ld_sc1(rs, WS_O + ((h * 16 + db) * 256 + lane + 64 * k) * 16);
    const u32x4 ml = ld_sc1(rs, WS_ML + (h * 32 + (lane & 31)) * 16);
    bool ok = (ml[2] == tagA) && (ml[3] == tagB);
#pragma unroll
    for (int i = 0; i < 4; ++i) ok = ok && (v[i][2] == tagA) && (v[i][3] == tagB);
    if (__all(ok) || spin > SPIN_CAP) {
      const float m = asf(ml[0]), l = asf(ml[1]);
      const float M = half_max(m);
      const float w = __builtin_amdgcn_exp2f(m - M);
      const float L = half_sum(w * l);
      float o0 = 0.f, o1 = 0.f;
#pragma unroll
      for (int k = 0; k < 4; ++k) {
        const float wk = __shfl(w, (lane >> 3) + 8 * k);
        o0 = fmaf(wk, asf(v[k][0]), o0); o1 = fmaf(wk, asf(v[k][1]), o1);
      }
      o0 += dpp<ROR8>(o0); o1 += dpp<ROR8>(o1);
      o0 = swap16_add(o0, o0); o1 = swap16_add(o1, o1);
      o0 = swap32_add(o0, o0); o1 = swap32_add(o1, o1);
      const float inv = 1.f / L;
      opk = cvtpk(o0 * inv, o1 * inv);
      break;
    }
    __builtin_amdgcn_s_sleep(1);
  }
#pragma unroll
  for (int p = 0; p < 8; ++p) {
    const unsigned os = __builtin_amdgcn_readlane(opk, p);
#pragma unroll
    for (int e = 0; e < 8; ++e) po[e] = dot2(wpk[p][e], os, po[e]);
  }
  if (live) {
#pragma unroll
    for (int k = 0; k < 4; ++k) st_sc1(rs, WS_P + ((lane * 256 + b) * 4 + k) * 16, u32x4{asu(po[2 * k]), asu(po[2 * k + 1]), tagA, tagB});
  }
  if (b >= 56) return;
  for (unsigned spin = 0;; ++spin) {
    u32x4 v[16];
#pragma unroll
    for (int k = 0; k < 16; ++k) v[k] = ld_sc1(rs, WS_P + (b * 1024 + lane + 64 * k) * 16);
    bool ok = true;
#pragma unroll
    for (int i = 0; i < 16; ++i) ok = ok && (v[i][2] == tagA) && (v[i][3] == tagB);
    if (__all(ok) || spin > SPIN_CAP) {
      float s0 = 0.f, s1 = 0.f;
#pragma unroll
      for (int k = 0; k < 16; ++k) { s0 += asf(v[k][0]); s1 += asf(v[k][1]); }
      s0 += dpp<ROR4>(s0); s1 += dpp<ROR4>(s1);
      s0 += dpp<ROR8>(s0); s1 += dpp<ROR8>(s1);
      s0 = swap16_add(s0, s0); s1 = swap16_add(s1, s1);
      s0 = swap32_add(s0, s0); s1 = swap32_add(s1, s1);
      const unsigned res = cvtpk(blo(xo) + s0 + b2f, bhi(xo) + s1 + b2f);
      if (lane < 4) *reinterpret_cast<unsigned*>(a.out + 8 * b + 2 * lane) = res;
      if (b == 0 && lane == 0) st_sc1(rs, WS_EPOCH, u32x4{tagA, tagB, 0u, 0u});
      break;
    }
    __builtin_amdgcn_s_sleep(1);
  }
}

extern "C" void kernel_launch(void* const* d_in, const int* in_sizes, int n_in,
                              void* d_out, int out_size, void* d_ws, size_t ws_size,
                              hipStream_t stream) {
  Args a;
  a.x = (const __hip_bfloat16*)d_in[0];
  a.kc = (const __hip_bfloat16*)d_in[1];
  a.vc = (const __hip_bfloat16*)d_in[2];
  a.wq = (const __hip_bfloat16*)d_in[3];
  a.wo = (const __hip_bfloat16*)d_in[4];
  a.w1 = (const __hip_bfloat16*)d_in[6];
  a.b1 = (const __hip_bfloat16*)d_in[7];
  a.w2 = (const __hip_bfloat16*)d_in[8];
  a.b2 = (const __hip_bfloat16*)d_in[9];
  a.out = (__hip_bfloat16*)d_out;
  a.ws = (unsigned char*)d_ws;
  fused_decode<<<256, 576, 0, stream>>>(a);
}
```

```cpp
#include <hip/hip_runtime.h>
#include <hip/hip_bf16.h>
#include <stdint.h>

typedef unsigned u32x4 __attribute__((ext_vector_type(4)));
typedef float f32x4 __attribute__((ext_vector_type(4)));
typedef float f32x2 __attribute__((ext_vector_type(2)));
typedef __bf16 bf16x2_t __attribute__((ext_vector_type(2)));

constexpr int D = 448, SK = 32768, WIN = 4096;
constexpr int WS_EPOCH = 0;
constexpr int WS_Q = 4096;
constexpr int WS_ML = WS_Q + 8 * 4 * 256 * 16;
constexpr int WS_O = WS_ML + 16 * 32 * 16;
constexpr int WS_P = WS_O + 16 * 16 * 256 * 16;
constexpr int WS_END = WS_P + 56 * 256 * 64;
constexpr unsigned SPIN_CAP = 20000;
#define LATEPOLL 1
#define VHEAD 4
#define VLEFT 2

struct Args {
  const __hip_bfloat16* x; const __hip_bfloat16* kc; const __hip_bfloat16* vc; const __hip_bfloat16* wq;
  const __hip_bfloat16* wo; const __hip_bfloat16* w1; const __hip_bfloat16* b1; const __hip_bfloat16* w2;
  const __hip_bfloat16* b2; __hip_bfloat16* out; unsigned char* ws;
};

__device__ __forceinline__ float asf(unsigned u) { return __builtin_bit_cast(float, u); }
__device__ __forceinline__ unsigned asu(float f) { return __builtin_bit_cast(unsigned, f); }
__device__ __forceinline__ float blo(unsigned u) { return asf(u << 16); }
__device__ __forceinline__ float bhi(unsigned u) { return asf(u & 0xffff0000u); }
__device__ __forceinline__ unsigned cvtpk(float lo, float hi) {
  bf16x2_t v; v[0] = (__bf16)lo; v[1] = (__bf16)hi; return __builtin_bit_cast(unsigned, v);
}
__device__ __forceinline__ float bfr(float x) { return blo(cvtpk(x, x)); }
__device__ __forceinline__ float dot2(unsigned a, unsigned b, float acc) {
  return __builtin_amdgcn_fdot2_f32_bf16(__builtin_bit_cast(bf16x2_t, a), __builtin_bit_cast(bf16x2_t, b), acc, false);
}
template <int CTRL> __device__ __forceinline__ float dpp(float x) {
  return __builtin_bit_cast(float, __builtin_amdgcn_mov_dpp(__builtin_bit_cast(int, x), CTRL, 0xf, 0xf, true));
}
constexpr int XOR1 = 0xB1, XOR2 = 0x4E, HMIR = 0x141, RMIR = 0x140, ROR8 = 0x128, ROR4 = 0x124;
__device__ __forceinline__ float swap16_add(float a, float b) {
  auto s = __builtin_amdgcn_permlane16_swap(asu(a), asu(b), false, false);
  return asf(s[0]) + asf(s[1]);
}
__device__ __forceinline__ float swap32_add(float a, float b) {
  auto s = __builtin_amdgcn_permlane32_swap(asu(a), asu(b), false, false);
  return asf(s[0]) + asf(s[1]);
}
__device__ __forceinline__ float swap16_max(float a) {
  auto s = __builtin_amdgcn_permlane16_swap(asu(a), asu(a), false, false);
  return fmaxf(asf(s[0]), asf(s[1]));
}
__device__ __forceinline__ float swap32_max(float a) {
  auto s = __builtin_amdgcn_permlane32_swap(asu(a), asu(a), false, false);
  return fmaxf(asf(s[0]), asf(s[1]));
}
__device__ __forceinline__ float wave_sum(float v) {
  v += dpp<XOR1>(v); v += dpp<XOR2>(v); v += dpp<HMIR>(v); v += dpp<RMIR>(v);
  v = swap16_add(v, v); v = swap32_add(v, v); return v;
}
__device__ __forceinline__ float half_max(float v) {
  v = fmaxf(v, dpp<XOR1>(v)); v = fmaxf(v, dpp<XOR2>(v)); v = fmaxf(v, dpp<HMIR>(v)); v = fmaxf(v, dpp<RMIR>(v));
  return swap16_max(v);
}
__device__ __forceinline__ float half_sum(float v) {
  v += dpp<XOR1>(v); v += dpp<XOR2>(v); v += dpp<HMIR>(v); v += dpp<RMIR>(v);
  return swap16_add(v, v);
}
__device__ __forceinline__ u32x4 ld_sc1(__amdgpu_buffer_rsrc_t r, int off, int soff = 0) { return __builtin_amdgcn_raw_buffer_load_b128(r, off, soff, 16); }
typedef __attribute__((address_space(3))) unsigned lds_u32;
__device__ __forceinline__ unsigned lds_flag_load(float* p) { return __hip_atomic_load((lds_u32*)p, __ATOMIC_RELAXED, __HIP_MEMORY_SCOPE_WORKGROUP); }
__device__ __forceinline__ void lds_flag_store(float* p, unsigned v) { __hip_atomic_store((lds_u32*)p, v, __ATOMIC_RELAXED, __HIP_MEMORY_SCOPE_WORKGROUP); }
__device__ __forceinline__ void st_sc1(__amdgpu_buffer_rsrc_t r, int off, u32x4 v) { __builtin_amdgcn_raw_buffer_store_b128(v, r, off, 0, 16); }
__device__ __forceinline__ u32x4 ldnt(const void* p) { return __builtin_nontemporal_load(reinterpret_cast<const u32x4*>(p)); }
#define STAMP(i) do {} while (0)
#define STAMP_DECL
#define STAMP_OUT(base) do {} while (0)
#define WG_BARRIER() asm volatile("s_waitcnt lgkmcnt(0)\n\ts_barrier" ::: "memory")
#define SCHED_FENCE() __builtin_amdgcn_sched_barrier(0)

constexpr int L_XS = 0;
constexpr int L_Q = 448;
constexpr int L_TAG = 704;
constexpr int L_FLG = 706;
constexpr int L_PRG = 707;
constexpr int L_MAX = 708;
constexpr int L_L = 724;
constexpr int L_P = 740;
constexpr int L_O = 1024;
constexpr int L_END = L_O + 8 * 512;

__device__ __forceinline__ void layernorm8(const float (&v)[8], bool live, float (&o)[8]) {
  float s = 0.f;
#pragma unroll
  for (int e = 0; e < 8; ++e) s += v[e];
  s = live ? s : 0.f;
  const float mean = wave_sum(s) * (1.f / 448.f);
  float q = 0.f;
#pragma unroll
  for (int e = 0; e < 8; ++e) { const float d = v[e] - mean; q = fmaf(d, d, q); }
  q = live ? q : 0.f;
  const float var = bfr(wave_sum(q) * (1.f / 448.f));
  const float mb = bfr(mean);
  const float den = bfr(__builtin_amdgcn_sqrtf(bfr(var + 1e-5f)));
  const float rden = __builtin_amdgcn_rcpf(den);
#pragma unroll
  for (int e = 0; e < 8; ++e) o[e] = bfr(bfr(v[e] - mb) * rden);
}

__global__ void __launch_bounds__(576, 3) fused_decode(Args a) {
  __shared__ __attribute__((aligned(16))) float lds[L_END];
  const int b = blockIdx.x, g = b & 7, s = b >> 3;
  const int lane = threadIdx.x & 63;
  const int wave = __builtin_amdgcn_readfirstlane(threadIdx.x >> 6);
  const __amdgpu_buffer_rsrc_t rs = __builtin_amdgcn_make_buffer_rsrc(a.ws, 0, WS_END, 0x00020000);
  float* const flg = &lds[L_FLG];

  if (wave < 8) {
    const int half = lane >> 5, c = lane & 31;
    STAMP_DECL; STAMP(0);
    const size_t base = ((size_t)g * SK + (SK - WIN) + s * 128 + wave * 16) * 256 + lane * 8;
    u32x4 K[8], V[8];
    WG_BARRIER();
#pragma unroll
    for (int i = 0; i < 8; ++i) K[i] = ldnt(a.kc + base + i * 512);
#pragma unroll
    for (int i = 0; i < VHEAD; ++i) V[i] = ldnt(a.vc + base + i * 512);
    SCHED_FENCE();
    asm volatile("s_waitcnt vmcnt(%0)" :: "n"(VLEFT) : "memory");
    if (lane == 0) __hip_atomic_fetch_add((lds_u32*)&lds[L_PRG], 1u, __ATOMIC_RELAXED, __HIP_MEMORY_SCOPE_WORKGROUP);
    while (lds_flag_load(flg) < 1u) __builtin_amdgcn_s_sleep(1);
#pragma unroll
    for (int i = VHEAD; i < 8; ++i) V[i] = ldnt(a.vc + base + i * 512);
    SCHED_FENCE();
    STAMP(1);
    while (lds_flag_load(flg) < 3u) __builtin_amdgcn_s_sleep(1);
    asm volatile("" ::: "memory");
    STAMP(2);
    const u32x4 q0 = *reinterpret_cast<const u32x4*>(&lds[L_Q + 4 * c]);
    const u32x4 q1 = *reinterpret_cast<const u32x4*>(&lds[L_Q + 128 + 4 * c]);
    const unsigned tagA = asu(lds[L_TAG]), tagB = asu(lds[L_TAG + 1]);
    float x[16];
#pragma unroll
    for (int i = 0; i < 8; ++i) {
      float d0 = 0.f, d1 = 0.f;
#pragma unroll
      for (int k = 0; k < 4; ++k) { d0 = dot2(K[i][k], q0[k], d0); d1 = dot2(K[i][k], q1[k], d1); }
      x[2 * i] = d0; x[2 * i + 1] = d1;
    }
#define TR_STEP(N, CTRL, BIT) _Pragma("unroll") for (int h = 0; h < (N); ++h) { \
      const float keep_ = (c & (BIT)) ? x[h + (N)] : x[h], send_ = (c & (BIT)) ? x[h] : x[h + (N)]; \
      x[h] = keep_ + dpp<CTRL>(send_); }
    TR_STEP(8, ROR8, 8) TR_STEP(4, HMIR, 4) TR_STEP(2, XOR2, 2) TR_STEP(1, XOR1, 1)
#undef TR_STEP
    float sc = swap16_add(x[0], x[0]);
    sc = bfr(sc) * (0.0625f * 1.4426950408889634f);
    float wm = sc;
    wm = fmaxf(wm, dpp<ROR4>(wm)); wm = fmaxf(wm, dpp<ROR8>(wm)); wm = fmaxf(wm, dpp<XOR2>(wm)); wm = swap32_max(wm);
    if (lane < 2) lds[L_MAX + wave * 2 + lane] = wm;
    STAMP(3);
    WG_BARRIER();
    STAMP(4);
    float M = lds[L_MAX + (c & 1)];
#pragma unroll
    for (int w = 1; w < 8; ++w) M = fmaxf(M, lds[L_MAX + w * 2 + (c & 1)]);
    const float p = __builtin_amdgcn_exp2f(sc - M);
    float ls = p;
    ls += dpp<ROR4>(ls); ls += dpp<ROR8>(ls); ls += dpp<XOR2>(ls); ls = swap32_add(ls, ls);
    if (lane < 2) lds[L_L + wave * 2 + lane] = ls;
    float* prow = &lds[L_P + (wave * 2 + half) * 16];
    if (c < 16) prow[c] = p;
    f32x4 pr[4];
#pragma unroll
    for (int u = 0; u < 4; ++u) pr[u] = *reinterpret_cast<const f32x4*>(prow + 4 * u);
    float acc[16];
#pragma unroll
    for (int k = 0; k < 16; ++k) acc[k] = 0.f;
#pragma unroll
    for (int i = 0; i < 8; ++i) {
      const float p0 = pr[i >> 1][(2 * i) & 3], p1 = pr[i >> 1][(2 * i + 1) & 3];
#pragma unroll
      for (int k = 0; k < 4; ++k) {
        const float lo = blo(V[i][k]), hi = bhi(V[i][k]);
        acc[2 * k] = fmaf(p0, lo, acc[2 * k]); acc[2 * k + 1] = fmaf(p0, hi, acc[2 * k + 1]);
        acc[8 + 2 * k] = fmaf(p1, lo, acc[8 + 2 * k]); acc[8 + 2 * k + 1] = fmaf(p1, hi, acc[8 + 2 * k + 1]);
      }
    }
    float r[8];
#pragma unroll
    for (int k = 0; k < 8; ++k) r[k] = swap32_add(acc[k], acc[8 + k]);
    float* orow = &lds[L_O + wave * 512 + half * 256 + 8 * c];
    *reinterpret_cast<f32x4*>(orow) = f32x4{r[0], r[1], r[2], r[3]};
    *reinterpret_cast<f32x4*>(orow + 4) = f32x4{r[4], r[5], r[6], r[7]};
    STAMP(5);
    WG_BARRIER();
    STAMP(6);
    if (wave < 4) {
      const int t = wave * 64 + lane, hd = t >> 7, pi = t & 127;
      f32x2 o = *reinterpret_cast<const f32x2*>(&lds[L_O + 2 * t]);
#pragma unroll
      for (int w = 1; w < 8; ++w) { const f32x2 u = *reinterpret_cast<const f32x2*>(&lds[L_O + w * 512 + 2 * t]); o[0] += u[0]; o[1] += u[1]; }
      st_sc1(rs, WS_O + ((((2 * g + hd) * 16 + (pi >> 3)) * 32 + s) * 8 + (pi & 7)) * 16, u32x4{asu(o[0]), asu(o[1]), tagA, tagB});
      if (wave == 0 && lane < 2) {
        float L = 0.f;
#pragma unroll
        for (int w = 0; w < 8; ++w) L += lds[L_L + w * 2 + lane];
        st_sc1(rs, WS_ML + ((2 * g + lane) * 32 + s) * 16, u32x4{asu(M), asu(L), tagA, tagB});
      }
      asm volatile("s_waitcnt vmcnt(0)" ::: "memory");
      if (lane == 0) __hip_atomic_fetch_add((lds_u32*)&lds[L_PRG], 1u, __ATOMIC_RELAXED, __HIP_MEMORY_SCOPE_WORKGROUP);
    }
    STAMP(7);
    if (wave == 0) STAMP_OUT(8);
    return;
  }

  STAMP_DECL; STAMP(0);
  const bool live = lane < 56;
  const int lc = live ? lane : 0;
  const u32x4 ep = ld_sc1(rs, WS_EPOCH);
  const u32x4 xraw = *reinterpret_cast<const u32x4*>(a.x + 8 * lc);
  const int kq = s >> 3, cb = s & 7;
  u32x4 wqv[14];
  {
    const __hip_bfloat16* wp = a.wq + (size_t)(kq * 112 + (lane >> 3)) * 4096 + g * 512 + cb * 64 + 8 * (lane & 7);
#pragma unroll
    for (int i = 0; i < 14; ++i) wqv[i] = ldnt(wp + (size_t)i * 8 * 4096);
  }
  const int fj = 7 * b + ((lane & 7) < 7 ? (lane & 7) : 6);
  const unsigned w1r = reinterpret_cast<const unsigned short*>(a.w1)[fj];
  const unsigned b1r = reinterpret_cast<const unsigned short*>(a.b1)[fj];
  const unsigned w2r = reinterpret_cast<const unsigned short*>(a.w2)[fj];
  if (lane == 0) { lds_flag_store(flg, 0u); lds_flag_store(&lds[L_PRG], 0u); }
  SCHED_FENCE();
  WG_BARRIER();
  SCHED_FENCE();
  float xv[8], xn[8], hh[8];
#pragma unroll
  for (int k = 0; k < 4; ++k) { xv[2 * k] = blo(xraw[k]); xv[2 * k + 1] = bhi(xraw[k]); }
  layernorm8(xv, live, xn);
  if (live) {
    *reinterpret_cast<f32x4*>(&lds[L_XS + 8 * lane]) = f32x4{xn[0], xn[1], xn[2], xn[3]};
    *reinterpret_cast<f32x4*>(&lds[L_XS + 8 * lane + 4]) = f32x4{xn[4], xn[5], xn[6], xn[7]};
  }
  const unsigned tagA = ep[0] + 1u, tagB = ep[1] + 0x9E3779B9u;
  STAMP(1);
  {
    float qa[8];
#pragma unroll
    for (int e = 0; e < 8; ++e) qa[e] = 0.f;
#pragma unroll
    for (int i = 0; i < 14; ++i) {
      const float xs = lds[L_XS + kq * 112 + 8 * i + (lane >> 3)];
#pragma unroll
      for (int k = 0; k < 4; ++k) { qa[2 * k] = fmaf(xs, blo(wqv[i][k]), qa[2 * k]); qa[2 * k + 1] = fmaf(xs, bhi(wqv[i][k]), qa[2 * k + 1]); }
    }
#pragma unroll
    for (int h = 0; h < 4; ++h) {
      const float keep = (lane & 8) ? qa[h + 4] : qa[h], send = (lane & 8) ? qa[h] : qa[h + 4];
      qa[h] = keep + dpp<ROR8>(send);
    }
    qa[0] = swap16_add(qa[0], qa[2]); qa[1] = swap16_add(qa[1], qa[3]);
    qa[0] = swap32_add(qa[0], qa[0]); qa[1] = swap32_add(qa[1], qa[1]);
    if (lane < 32) {
      const int pidx = cb * 32 + 4 * (lane & 7) + 2 * ((lane >> 3) & 1) + ((lane >> 4) & 1);
      st_sc1(rs, WS_Q + ((g * 4 + kq) * 256 + pidx) * 16, u32x4{asu(qa[0]), asu(qa[1]), tagA, tagB});
    }
  }
  STAMP(2);
  SCHED_FENCE();
  layernorm8(xn, live, hh);
  float po[8];
  {
    float w1f[7], b1f[7], w2f[7];
#pragma unroll
    for (int j = 0; j < 7; ++j) {
      w1f[j] = blo(__builtin_amdgcn_readlane(w1r, j)); b1f[j] = blo(__builtin_amdgcn_readlane(b1r, j)); w2f[j] = blo(__builtin_amdgcn_readlane(w2r, j));
    }
#pragma unroll
    for (int e = 0; e < 8; ++e) po[e] = 0.f;
#pragma unroll
    for (int j = 0; j < 7; ++j) {
#pragma unroll
      for (int e = 0; e < 8; ++e) {
        const float u = fmaf(hh[e], w1f[j], b1f[j]);
        const float z = u * (1.f + 0.044715f * u * u) * (-2.f * 0.7978845608028654f * 1.4426950408889634f);
        const float gl = u * __builtin_amdgcn_rcpf(1.f + __builtin_amdgcn_exp2f(z));
        po[e] = fmaf(gl, w2f[j], po[e]);
      }
    }
#pragma unroll
    for (int e = 0; e < 8; ++e) asm volatile("" : "+v"(po[e]));
  }
  SCHED_FENCE();
  while (lds_flag_load(&lds[L_PRG]) < 8u) __builtin_amdgcn_s_sleep(1);
  for (unsigned spin = 0;; ++spin) {
    u32x4 v[16];
#pragma unroll
    for (int j = 0; j < 4; ++j)
#pragma unroll
      for (int k = 0; k < 4; ++k) v[j * 4 + k] = ld_sc1(rs, lane * 16, WS_Q + ((g * 4 + k) * 256 + 64 * j) * 16);
    SCHED_FENCE();
    if (lane == 0) lds_flag_store(flg, 1u);
    SCHED_FENCE();
    bool ok = true;
#pragma unroll
    for (int i = 0; i < 16; ++i) ok = ok && (v[i][2] == tagA) && (v[i][3] == tagB);
    if (__all(ok) || spin > SPIN_CAP) {
#pragma unroll
      for (int j = 0; j < 4; ++j) {
        const float s0 = (asf(v[j * 4][0]) + asf(v[j * 4 + 1][0])) + (asf(v[j * 4 + 2][0]) + asf(v[j * 4 + 3][0]));
        const float s1 = (asf(v[j * 4][1]) + asf(v[j * 4 + 1][1])) + (asf(v[j * 4 + 2][1]) + asf(v[j * 4 + 3][1]));
        lds[L_Q + lane + 64 * j] = asf(cvtpk(s0, s1));
      }
      break;
    }
  }
  STAMP(3);
  if (lane == 0) { lds[L_TAG] = asf(tagA); lds[L_TAG + 1] = asf(tagB); }
  asm volatile("s_waitcnt lgkmcnt(0)" ::: "memory");
  if (lane == 0) lds_flag_store(flg, 3u);
  SCHED_FENCE();
  const int h = 2 * g + (s >> 4), db = s & 15;
  u32x4 wov[16];
  {
    const __hip_bfloat16* wp = a.wo + (size_t)(h * 256 + db * 16) * D + 8 * lc;
#pragma unroll
    for (int r = 0; r < 16; ++r) wov[r] = ldnt(wp + r * D);
  }
  unsigned xo = 0, b2r = 0;
  if (b < 56) { xo = *reinterpret_cast<const unsigned*>(a.x + 8 * b + 2 * (lane & 3)); b2r = reinterpret_cast<const unsigned short*>(a.b2)[0]; }
  SCHED_FENCE();
  WG_BARRIER();
  unsigned wpk[8][8];
#pragma unroll
  for (int p = 0; p < 8; ++p)
#pragma unroll
    for (int k = 0; k < 4; ++k) {
      wpk[p][2 * k] = __builtin_amdgcn_perm(wov[2 * p + 1][k], wov[2 * p][k], 0x05040100u);
      wpk[p][2 * k + 1] = __builtin_amdgcn_perm(wov[2 * p + 1][k], wov[2 * p][k], 0x07060302u);
    }
#pragma unroll
  for (int p = 0; p < 8; ++p)
#pragma unroll
    for (int e = 0; e < 8; ++e) asm volatile("" : "+v"(wpk[p][e]));
  SCHED_FENCE();
  WG_BARRIER();
  while (lds_flag_load(&lds[L_PRG]) < 12u) __builtin_amdgcn_s_sleep(1);
  STAMP(4);
  unsigned opk = 0;
  {
    u32x4 R[5];
#define S2_ISSUE(P) do { _Pragma("unroll") for (int k = 0; k < 4; ++k) P[k] = ld_sc1(rs, lane * 16, WS_O + ((h * 16 + db) * 256 + 64 * k) * 16); \
      P[4] = ld_sc1(rs, (lane & 31) * 16, WS_ML + h * 32 * 16); SCHED_FENCE(); } while (0)
#define S2_CHECK(P) { bool ok = true; _Pragma("unroll") for (int i = 0; i < 5; ++i) ok = ok && (P[i][2] == tagA) && (P[i][3] == tagB); \
      if (__all(ok) || spin > SPIN_CAP) { _Pragma("unroll") for (int i = 0; i < 5; ++i) R[i] = P[i]; break; } }
    u32x4 A[5], B[5];
    S2_ISSUE(A); S2_ISSUE(B);
    for (unsigned spin = 0;; ++spin) {
      S2_CHECK(A) S2_ISSUE(A); S2_CHECK(B) S2_ISSUE(B);
    }
#undef S2_ISSUE
#undef S2_CHECK
    const float m = asf(R[4][0]), l = asf(R[4][1]);
    const float M = half_max(m);
    const float w = __builtin_amdgcn_exp2f(m - M);
    const float L = half_sum(w * l);
    float o0 = 0.f, o1 = 0.f;
#pragma unroll
    for (int k = 0; k < 4; ++k) {
      const float wk = __shfl(w, (lane >> 3) + 8 * k);
      o0 = fmaf(wk, asf(R[k][0]), o0); o1 = fmaf(wk, asf(R[k][1]), o1);
    }
    o0 += dpp<ROR8>(o0); o1 += dpp<ROR8>(o1);
    o0 = swap16_add(o0, o0); o1 = swap16_add(o1, o1);
    o0 = swap32_add(o0, o0); o1 = swap32_add(o1, o1);
    const float inv = __builtin_amdgcn_rcpf(L);
    opk = cvtpk(o0 * inv, o1 * inv);
  }
  STAMP(5);
#pragma unroll
  for (int p = 0; p < 8; ++p) {
    const unsigned os = __builtin_amdgcn_readlane(opk, p);
#pragma unroll
    for (int e = 0; e < 8; ++e) po[e] = dot2(wpk[p][e], os, po[e]);
  }
  if (live) {
#pragma unroll
    for (int k = 0; k < 4; ++k) st_sc1(rs, WS_P + ((lane * 256 + b) * 4 + k) * 16, u32x4{asu(po[2 * k]), asu(po[2 * k + 1]), tagA, tagB});
  }
  STAMP(6);
  if (b >= 56) { STAMP_OUT(0); return; }
  asm volatile("s_waitcnt vmcnt(0)" ::: "memory");
  {
    float s0 = 0.f, s1 = 0.f;
#define S3_ISSUE(P) do { _Pragma("unroll") for (int k = 0; k < 16; ++k) P[k] = ld_sc1(rs, lane * 16, WS_P + (b * 1024 + 64 * k) * 16); \
      SCHED_FENCE(); } while (0)
#define S3_CHECK(P) { bool ok = true; _Pragma("unroll") for (int i = 0; i < 16; ++i) ok = ok && (P[i][2] == tagA) && (P[i][3] == tagB); \
      if (__all(ok) || spin > SPIN_CAP) { _Pragma("unroll") for (int k = 0; k < 16; ++k) { s0 += asf(P[k][0]); s1 += asf(P[k][1]); } break; } }
    u32x4 A[16], B[16];
    S3_ISSUE(A); S3_ISSUE(B);
    for (unsigned spin = 0;; ++spin) {
      S3_CHECK(A) S3_ISSUE(A); S3_CHECK(B) S3_ISSUE(B);
    }
#undef S3_ISSUE
#undef S3_CHECK
    s0 += dpp<ROR4>(s0); s1 += dpp<ROR4>(s1);
    s0 += dpp<ROR8>(s0); s1 += dpp<ROR8>(s1);
    s0 = swap16_add(s0, s0); s1 = swap16_add(s1, s1);
    s0 = swap32_add(s0, s0); s1 = swap32_add(s1, s1);
    const float b2f = blo(b2r);
    const unsigned res = cvtpk(blo(xo) + s0 + b2f, bhi(xo) + s1 + b2f);
    if (lane < 4) *reinterpret_cast<unsigned*>(a.out + 8 * b + 2 * lane) = res;
    if (b == 0 && lane == 0) st_sc1(rs, WS_EPOCH, u32x4{tagA, tagB, 0u, 0u});
  }
  STAMP(7);
  STAMP_OUT(0);
}

extern "C" void kernel_launch(void* const* d_in, const int* in_sizes, int n_in,
                              void* d_out, int out_size, void* d_ws, size_t ws_size,
                              hipStream_t stream) {
  Args a;
  a.x = (const __hip_bfloat16*)d_in[0];
  a.kc = (const __hip_bfloat16*)d_in[1];
  a.vc = (const __hip_bfloat16*)d_in[2];
  a.wq = (const __hip_bfloat16*)d_in[3];
  a.wo = (const __hip_bfloat16*)d_in[4];
  a.w1 = (const __hip_bfloat16*)d_in[6];
  a.b1 = (const __hip_bfloat16*)d_in[7];
  a.w2 = (const __hip_bfloat16*)d_in[8];
  a.b2 = (const __hip_bfloat16*)d_in[9];
  a.out = (__hip_bfloat16*)d_out;
  a.ws = (unsigned char*)d_ws;
  fused_decode<<<256, 576, 0, stream>>>(a);
}
```

```cpp
#include <hip/hip_runtime.h>
#include <hip/hip_bf16.h>
#include <stdint.h>

typedef unsigned u32x4 __attribute__((ext_vector_type(4)));
typedef float f32x4 __attribute__((ext_vector_type(4)));
typedef float f32x2 __attribute__((ext_vector_type(2)));
typedef __bf16 bf16x2_t __attribute__((ext_vector_type(2)));

constexpr int D = 448, SK = 32768, WIN = 4096;
constexpr int WS_EPOCH = 0;
constexpr int WS_Q = 4096;
constexpr int WS_ML = WS_Q + 8 * 4 * 256 * 16;
constexpr int WS_O = WS_ML + 16 * 32 * 16;
constexpr int WS_P = WS_O + 16 * 16 * 256 * 16;
constexpr int WS_F = WS_P + 56 * 256 * 64;
constexpr int WS_END = WS_F + 256 * 16;
constexpr unsigned SPIN_CAP = 20000;
#define LATEPOLL 1
#define POLL_SLEEP 4
#define KFIRST 3
#define VHEAD 4
#define VLEFT 4

struct Args {
  const __hip_bfloat16* x; const __hip_bfloat16* kc; const __hip_bfloat16* vc; const __hip_bfloat16* wq;
  const __hip_bfloat16* wo; const __hip_bfloat16* w1; const __hip_bfloat16* b1; const __hip_bfloat16* w2;
  const __hip_bfloat16* b2; __hip_bfloat16* out; unsigned char* ws;
};

__device__ __forceinline__ float asf(unsigned u) { return __builtin_bit_cast(float, u); }
__device__ __forceinline__ unsigned asu(float f) { return __builtin_bit_cast(unsigned, f); }
__device__ __forceinline__ float blo(unsigned u) { return asf(u << 16); }
__device__ __forceinline__ float bhi(unsigned u) { return asf(u & 0xffff0000u); }
__device__ __forceinline__ unsigned cvtpk(float lo, float hi) {
  bf16x2_t v; v[0] = (__bf16)lo; v[1] = (__bf16)hi; return __builtin_bit_cast(unsigned, v);
}
__device__ __forceinline__ float bfr(float x) { return blo(cvtpk(x, x)); }
__device__ __forceinline__ float dot2(unsigned a, unsigned b, float acc) {
  return __builtin_amdgcn_fdot2_f32_bf16(__builtin_bit_cast(bf16x2_t, a), __builtin_bit_cast(bf16x2_t, b), acc, false);
}
template <int CTRL> __device__ __forceinline__ float dpp(float x) {
  return __builtin_bit_cast(float, __builtin_amdgcn_mov_dpp(__builtin_bit_cast(int, x), CTRL, 0xf, 0xf, true));
}
constexpr int XOR1 = 0xB1, XOR2 = 0x4E, HMIR = 0x141, RMIR = 0x140, ROR8 = 0x128, ROR4 = 0x124;
__device__ __forceinline__ float swap16_add(float a, float b) {
  auto s = __builtin_amdgcn_permlane16_swap(asu(a), asu(b), false, false);
  return asf(s[0]) + asf(s[1]);
}
__device__ __forceinline__ float swap32_add(float a, float b) {
  auto s = __builtin_amdgcn_permlane32_swap(asu(a), asu(b), false, false);
  return asf(s[0]) + asf(s[1]);
}
__device__ __forceinline__ float swap16_max(float a) {
  auto s = __builtin_amdgcn_permlane16_swap(asu(a), asu(a), false, false);
  return fmaxf(asf(s[0]), asf(s[1]));
}
__device__ __forceinline__ float swap32_max(float a) {
  auto s = __builtin_amdgcn_permlane32_swap(asu(a), asu(a), false, false);
  return fmaxf(asf(s[0]), asf(s[1]));
}
__device__ __forceinline__ float wave_sum(float v) {
  v += dpp<XOR1>(v); v += dpp<XOR2>(v); v += dpp<HMIR>(v); v += dpp<RMIR>(v);
  v = swap16_add(v, v); v = swap32_add(v, v); return v;
}
__device__ __forceinline__ float half_max(float v) {
  v = fmaxf(v, dpp<XOR1>(v)); v = fmaxf(v, dpp<XOR2>(v)); v = fmaxf(v, dpp<HMIR>(v)); v = fmaxf(v, dpp<RMIR>(v));
  return swap16_max(v);
}
__device__ __forceinline__ float half_sum(float v) {
  v += dpp<XOR1>(v); v += dpp<XOR2>(v); v += dpp<HMIR>(v); v += dpp<RMIR>(v);
  return swap16_add(v, v);
}
__device__ __forceinline__ u32x4 ld_sc1(__amdgpu_buffer_rsrc_t r, int off, int soff = 0) { return __builtin_amdgcn_raw_buffer_load_b128(r, off, soff, 16); }
typedef __attribute__((address_space(3))) unsigned lds_u32;
__device__ __forceinline__ unsigned lds_flag_load(float* p) { return __hip_atomic_load((lds_u32*)p, __ATOMIC_RELAXED, __HIP_MEMORY_SCOPE_WORKGROUP); }
__device__ __forceinline__ void lds_flag_store(float* p, unsigned v) { __hip_atomic_store((lds_u32*)p, v, __ATOMIC_RELAXED, __HIP_MEMORY_SCOPE_WORKGROUP); }
__device__ __forceinline__ void st_sc1(__amdgpu_buffer_rsrc_t r, int off, u32x4 v) { __builtin_amdgcn_raw_buffer_store_b128(v, r, off, 0, 16); }
__device__ __forceinline__ u32x4 ldnt(const void* p) { return __builtin_nontemporal_load(reinterpret_cast<const u32x4*>(p)); }
#define STAMP(i) do {} while (0)
#define STAMP_DECL
#define STAMP_OUT(base) do {} while (0)
#define WG_BARRIER() asm volatile("s_waitcnt lgkmcnt(0)\n\ts_barrier" ::: "memory")
#define SCHED_FENCE() __builtin_amdgcn_sched_barrier(0)

constexpr int L_XS = 0;
constexpr int L_Q = 448;
constexpr int L_TAG = 704;
constexpr int L_FLG = 706;
constexpr int L_PRG = 707;
constexpr int L_PUB = 1020;
constexpr int L_MAX = 708;
constexpr int L_L = 724;
constexpr int L_P = 740;
constexpr int L_O = 1024;
constexpr int L_END = L_O + 8 * 512;

__device__ __forceinline__ void layernorm8(const float (&v)[8], bool live, float (&o)[8]) {
  float s = 0.f;
#pragma unroll
  for (int e = 0; e < 8; ++e) s += v[e];
  s = live ? s : 0.f;
  const float mean = wave_sum(s) * (1.f / 448.f);
  float q = 0.f;
#pragma unroll
  for (int e = 0; e < 8; ++e) { const float d = v[e] - mean; q = fmaf(d, d, q); }
  q = live ? q : 0.f;
  const float var = bfr(wave_sum(q) * (1.f / 448.f));
  const float mb = bfr(mean);
  const float den = bfr(__builtin_amdgcn_sqrtf(bfr(var + 1e-5f)));
  const float rden = __builtin_amdgcn_rcpf(den);
#pragma unroll
  for (int e = 0; e < 8; ++e) o[e] = bfr(bfr(v[e] - mb) * rden);
}

__global__ void __launch_bounds__(576, 3) fused_decode(Args a) {
  __shared__ __attribute__((aligned(16))) float lds[L_END];
  const int b = blockIdx.x, g = b & 7, s = b >> 3;
  const int lane = threadIdx.x & 63;
  const int wave = __builtin_amdgcn_readfirstlane(threadIdx.x >> 6);
  const __amdgpu_buffer_rsrc_t rs = __builtin_amdgcn_make_buffer_rsrc(a.ws, 0, WS_END, 0x00020000);
  float* const flg = &lds[L_FLG];

  if (wave < 8) {
    const int half = lane >> 5, c = lane & 31;
    STAMP_DECL; STAMP(0);
    const size_t base = ((size_t)g * SK + (SK - WIN) + s * 128 + wave * 16) * 256 + lane * 8;
    u32x4 K[8], V[8];
    WG_BARRIER();
#pragma unroll
    for (int i = 0; i < KFIRST; ++i) K[i] = ldnt(a.kc + base + i * 512);
    SCHED_FENCE();
    while (lds_flag_load(&lds[L_PUB]) == 0u) __builtin_amdgcn_s_sleep(1);
#pragma unroll
    for (int i = KFIRST; i < 8; ++i) K[i] = ldnt(a.kc + base + i * 512);
#pragma unroll
    for (int i = 0; i < VHEAD; ++i) V[i] = ldnt(a.vc + base + i * 512);
    SCHED_FENCE();
    asm volatile("s_waitcnt vmcnt(%0)" :: "n"(VLEFT) : "memory");
    if (lane == 0) __hip_atomic_fetch_add((lds_u32*)&lds[L_PRG], 1u, __ATOMIC_RELAXED, __HIP_MEMORY_SCOPE_WORKGROUP);
    while (lds_flag_load(flg) < 1u) __builtin_amdgcn_s_sleep(1);
#pragma unroll
    for (int i = VHEAD; i < 8; ++i) V[i] = ldnt(a.vc + base + i * 512);
    SCHED_FENCE();
    STAMP(1);
    while (lds_flag_load(flg) < 3u) __builtin_amdgcn_s_sleep(1);
    asm volatile("" ::: "memory");
    STAMP(2);
    const u32x4 q0 = *reinterpret_cast<const u32x4*>(&lds[L_Q + 4 * c]);
    const u32x4 q1 = *reinterpret_cast<const u32x4*>(&lds[L_Q + 128 + 4 * c]);
    const unsigned tagA = asu(lds[L_TAG]), tagB = asu(lds[L_TAG + 1]);
    float x[16];
#pragma unroll
    for (int i = 0; i < 8; ++i) {
      float d0 = 0.f, d1 = 0.f;
#pragma unroll
      for (int k = 0; k < 4; ++k) { d0 = dot2(K[i][k], q0[k], d0); d1 = dot2(K[i][k], q1[k], d1); }
      x[2 * i] = d0; x[2 * i + 1] = d1;
    }
#define TR_STEP(N, CTRL, BIT) _Pragma("unroll") for (int h = 0; h < (N); ++h) { \
      const float keep_ = (c & (BIT)) ? x[h + (N)] : x[h], send_ = (c & (BIT)) ? x[h] : x[h + (N)]; \
      x[h] = keep_ + dpp<CTRL>(send_); }
    TR_STEP(8, ROR8, 8) TR_STEP(4, HMIR, 4) TR_STEP(2, XOR2, 2) TR_STEP(1, XOR1, 1)
#undef TR_STEP
    float sc = swap16_add(x[0], x[0]);
    sc = bfr(sc) * (0.0625f * 1.4426950408889634f);
    float wm = sc;
    wm = fmaxf(wm, dpp<ROR4>(wm)); wm = fmaxf(wm, dpp<ROR8>(wm)); wm = fmaxf(wm, dpp<XOR2>(wm)); wm = swap32_max(wm);
    if (lane < 2) lds[L_MAX + wave * 2 + lane] = wm;
    STAMP(3);
    WG_BARRIER();
    STAMP(4);
    float M = lds[L_MAX + (c & 1)];
#pragma unroll
    for (int w = 1; w < 8; ++w) M = fmaxf(M, lds[L_MAX + w * 2 + (c & 1)]);
    const float p = __builtin_amdgcn_exp2f(sc - M);
    float ls = p;
    ls += dpp<ROR4>(ls); ls += dpp<ROR8>(ls); ls += dpp<XOR2>(ls); ls = swap32_add(ls, ls);
    if (lane < 2) lds[L_L + wave * 2 + lane] = ls;
    float* prow = &lds[L_P + (wave * 2 + half) * 16];
    if (c < 16) prow[c] = p;
    f32x4 pr[4];
#pragma unroll
    for (int u = 0; u < 4; ++u) pr[u] = *reinterpret_cast<const f32x4*>(prow + 4 * u);
    float acc[16];
#pragma unroll
    for (int k = 0; k < 16; ++k) acc[k] = 0.f;
#pragma unroll
    for (int i = 0; i < 8; ++i) {
      const float p0 = pr[i >> 1][(2 * i) & 3], p1 = pr[i >> 1][(2 * i + 1) & 3];
#pragma unroll
      for (int k = 0; k < 4; ++k) {
        const float lo = blo(V[i][k]), hi = bhi(V[i][k]);
        acc[2 * k] = fmaf(p0, lo, acc[2 * k]); acc[2 * k + 1] = fmaf(p0, hi, acc[2 * k + 1]);
        acc[8 + 2 * k] = fmaf(p1, lo, acc[8 + 2 * k]); acc[8 + 2 * k + 1] = fmaf(p1, hi, acc[8 + 2 * k + 1]);
      }
    }
    float r[8];
#pragma unroll
    for (int k = 0; k < 8; ++k) r[k] = swap32_add(acc[k], acc[8 + k]);
    float* orow = &lds[L_O + wave * 512 + half * 256 + 8 * c];
    *reinterpret_cast<f32x4*>(orow) = f32x4{r[0], r[1], r[2], r[3]};
    *reinterpret_cast<f32x4*>(orow + 4) = f32x4{r[4], r[5], r[6], r[7]};
    STAMP(5);
    WG_BARRIER();
    STAMP(6);
    if (wave < 4) {
      const int t = wave * 64 + lane, hd = t >> 7, pi = t & 127;
      f32x2 o = *reinterpret_cast<const f32x2*>(&lds[L_O + 2 * t]);
#pragma unroll
      for (int w = 1; w < 8; ++w) { const f32x2 u = *reinterpret_cast<const f32x2*>(&lds[L_O + w * 512 + 2 * t]); o[0] += u[0]; o[1] += u[1]; }
      st_sc1(rs, WS_O + ((((2 * g + hd) * 16 + (pi >> 3)) * 32 + s) * 8 + (pi & 7)) * 16, u32x4{asu(o[0]), asu(o[1]), tagA, tagB});
      if (wave == 0 && lane < 2) {
        float L = 0.f;
#pragma unroll
        for (int w = 0; w < 8; ++w) L += lds[L_L + w * 2 + lane];
        st_sc1(rs, WS_ML + ((2 * g + lane) * 32 + s) * 16, u32x4{asu(M), asu(L), tagA, tagB});
      }
      asm volatile("s_waitcnt vmcnt(0)" ::: "memory");
      if (lane == 0) __hip_atomic_fetch_add((lds_u32*)&lds[L_PRG], 1u, __ATOMIC_RELAXED, __HIP_MEMORY_SCOPE_WORKGROUP);
    }
    STAMP(7);
    if (wave == 0) STAMP_OUT(8);
    return;
  }

  STAMP_DECL; STAMP(0);
  const bool live = lane < 56;
  const int lc = live ? lane : 0;
  const u32x4 ep = ld_sc1(rs, WS_EPOCH);
  const u32x4 xraw = *reinterpret_cast<const u32x4*>(a.x + 8 * lc);
  const int kq = s >> 3, cb = s & 7;
  u32x4 wqv[14];
  {
    const __hip_bfloat16* wp = a.wq + (size_t)(kq * 112 + (lane >> 3)) * 4096 + g * 512 + cb * 64 + 8 * (lane & 7);
#pragma unroll
    for (int i = 0; i < 14; ++i) wqv[i] = ldnt(wp + (size_t)i * 8 * 4096);
  }
  const int fj = 7 * b + ((lane & 7) < 7 ? (lane & 7) : 6);
  const unsigned w1r = reinterpret_cast<const unsigned short*>(a.w1)[fj];
  const unsigned b1r = reinterpret_cast<const unsigned short*>(a.b1)[fj];
  const unsigned w2r = reinterpret_cast<const unsigned short*>(a.w2)[fj];
  if (lane == 0) { lds_flag_store(flg, 0u); lds_flag_store(&lds[L_PRG], 0u); lds_flag_store(&lds[L_PUB], 0u); }
  SCHED_FENCE();
  WG_BARRIER();
  SCHED_FENCE();
  float xv[8], xn[8], hh[8];
#pragma unroll
  for (int k = 0; k < 4; ++k) { xv[2 * k] = blo(xraw[k]); xv[2 * k + 1] = bhi(xraw[k]); }
  layernorm8(xv, live, xn);
  if (live) {
    *reinterpret_cast<f32x4*>(&lds[L_XS + 8 * lane]) = f32x4{xn[0], xn[1], xn[2], xn[3]};
    *reinterpret_cast<f32x4*>(&lds[L_XS + 8 * lane + 4]) = f32x4{xn[4], xn[5], xn[6], xn[7]};
  }
  const unsigned tagA = ep[0] + 1u, tagB = ep[1] + 0x9E3779B9u;
  STAMP(1);
  {
    float qa[8];
#pragma unroll
    for (int e = 0; e < 8; ++e) qa[e] = 0.f;
#pragma unroll
    for (int i = 0; i < 14; ++i) {
      const float xs = lds[L_XS + kq * 112 + 8 * i + (lane >> 3)];
#pragma unroll
      for (int k = 0; k < 4; ++k) { qa[2 * k] = fmaf(xs, blo(wqv[i][k]), qa[2 * k]); qa[2 * k + 1] = fmaf(xs, bhi(wqv[i][k]), qa[2 * k + 1]); }
    }
#pragma unroll
    for (int h = 0; h < 4; ++h) {
      const float keep = (lane & 8) ? qa[h + 4] : qa[h], send = (lane & 8) ? qa[h] : qa[h + 4];
      qa[h] = keep + dpp<ROR8>(send);
    }
    qa[0] = swap16_add(qa[0], qa[2]); qa[1] = swap16_add(qa[1], qa[3]);
    qa[0] = swap32_add(qa[0], qa[0]); qa[1] = swap32_add(qa[1], qa[1]);
    if (lane < 32) {
      const int pidx = cb * 32 + 4 * (lane & 7) + 2 * ((lane >> 3) & 1) + ((lane >> 4) & 1);
      st_sc1(rs, WS_Q + ((g * 4 + kq) * 256 + pidx) * 16, u32x4{asu(qa[0]), asu(qa[1]), tagA, tagB});
    }
  }
  SCHED_FENCE();
  if (lane == 0) lds_flag_store(&lds[L_PUB], 1u);
  STAMP(2);
  SCHED_FENCE();
  layernorm8(xn, live, hh);
  float po[8];
  {
    float w1f[7], b1f[7], w2f[7];
#pragma unroll
    for (int j = 0; j < 7; ++j) {
      w1f[j] = blo(__builtin_amdgcn_readlane(w1r, j)); b1f[j] = blo(__builtin_amdgcn_readlane(b1r, j)); w2f[j] = blo(__builtin_amdgcn_readlane(w2r, j));
    }
#pragma unroll
    for (int e = 0; e < 8; ++e) po[e] = 0.f;
#pragma unroll
    for (int j = 0; j < 7; ++j) {
#pragma unroll
      for (int e = 0; e < 8; ++e) {
        const float u = fmaf(hh[e], w1f[j], b1f[j]);
        const float z = u * (1.f + 0.044715f * u * u) * (-2.f * 0.7978845608028654f * 1.4426950408889634f);
        const float gl = u * __builtin_amdgcn_rcpf(1.f + __builtin_amdgcn_exp2f(z));
        po[e] = fmaf(gl, w2f[j], po[e]);
      }
    }
#pragma unroll
    for (int e = 0; e < 8; ++e) asm volatile("" : "+v"(po[e]));
  }
  SCHED_FENCE();
  while (lds_flag_load(&lds[L_PRG]) < 8u) __builtin_amdgcn_s_sleep(1);
  for (unsigned spin = 0;; ++spin) {
    u32x4 v[16];
#pragma unroll
    for (int j = 0; j < 4; ++j)
#pragma unroll
      for (int k = 0; k < 4; ++k) v[j * 4 + k] = ld_sc1(rs, lane * 16, WS_Q + ((g * 4 + k) * 256 + 64 * j) * 16);
    SCHED_FENCE();
    if (lane == 0) lds_flag_store(flg, 1u);
    SCHED_FENCE();
    bool ok = true;
#pragma unroll
    for (int i = 0; i < 16; ++i) ok = ok && (v[i][2] == tagA) && (v[i][3] == tagB);
    if (__all(ok) || spin > SPIN_CAP) {
#pragma unroll
      for (int j = 0; j < 4; ++j) {
        const float s0 = (asf(v[j * 4][0]) + asf(v[j * 4 + 1][0])) + (asf(v[j * 4 + 2][0]) + asf(v[j * 4 + 3][0]));
        const float s1 = (asf(v[j * 4][1]) + asf(v[j * 4 + 1][1])) + (asf(v[j * 4 + 2][1]) + asf(v[j * 4 + 3][1]));
        lds[L_Q + lane + 64 * j] = asf(cvtpk(s0, s1));
      }
      break;
    }
  }
  STAMP(3);
  if (lane == 0) { lds[L_TAG] = asf(tagA); lds[L_TAG + 1] = asf(tagB); }
  asm volatile("s_waitcnt lgkmcnt(0)" ::: "memory");
  if (lane == 0) lds_flag_store(flg, 3u);
  SCHED_FENCE();
  const int h = 2 * g + (s >> 4), db = s & 15;
  u32x4 wov[16];
  {
    const __hip_bfloat16* wp = a.wo + (size_t)(h * 256 + db * 16) * D + 8 * lc;
#pragma unroll
    for (int r = 0; r < 16; ++r) wov[r] = ldnt(wp + r * D);
  }
  unsigned xo = 0, b2r = 0;
  if (b < 56) { xo = *reinterpret_cast<const unsigned*>(a.x + 8 * b + 2 * (lane & 3)); b2r = reinterpret_cast<const unsigned short*>(a.b2)[0]; }
  SCHED_FENCE();
  WG_BARRIER();
  unsigned wpk[8][8];
#pragma unroll
  for (int p = 0; p < 8; ++p)
#pragma unroll
    for (int k = 0; k < 4; ++k) {
      wpk[p][2 * k] = __builtin_amdgcn_perm(wov[2 * p + 1][k], wov[2 * p][k], 0x05040100u);
      wpk[p][2 * k + 1] = __builtin_amdgcn_perm(wov[2 * p + 1][k], wov[2 * p][k], 0x07060302u);
    }
#pragma unroll
  for (int p = 0; p < 8; ++p)
#pragma unroll
    for (int e = 0; e < 8; ++e) asm volatile("" : "+v"(wpk[p][e]));
  SCHED_FENCE();
  WG_BARRIER();
  while (lds_flag_load(&lds[L_PRG]) < 12u) __builtin_amdgcn_s_sleep(1);
  STAMP(4);
  unsigned opk = 0;
  {
    u32x4 R[5];
    for (unsigned spin = 0;; ++spin) {
      R[4] = ld_sc1(rs, (lane & 31) * 16, WS_ML + h * 32 * 16);
      if (__all((R[4][2] == tagA) && (R[4][3] == tagB)) || spin > SPIN_CAP) break;
      __builtin_amdgcn_s_sleep(POLL_SLEEP);
    }
    for (unsigned spin = 0;; ++spin) {
#pragma unroll
      for (int k = 0; k < 4; ++k) R[k] = ld_sc1(rs, lane * 16, WS_O + ((h * 16 + db) * 256 + 64 * k) * 16);
      bool ok = true;
#pragma unroll
      for (int i = 0; i < 4; ++i) ok = ok && (R[i][2] == tagA) && (R[i][3] == tagB);
      if (__all(ok) || spin > SPIN_CAP) break;
      __builtin_amdgcn_s_sleep(POLL_SLEEP);
    }
    const float m = asf(R[4][0]), l = asf(R[4][1]);
    const float M = half_max(m);
    const float w = __builtin_amdgcn_exp2f(m - M);
    const float L = half_sum(w * l);
    float o0 = 0.f, o1 = 0.f;
#pragma unroll
    for (int k = 0; k < 4; ++k) {
      const float wk = __shfl(w, (lane >> 3) + 8 * k);
      o0 = fmaf(wk, asf(R[k][0]), o0); o1 = fmaf(wk, asf(R[k][1]), o1);
    }
    o0 += dpp<ROR8>(o0); o1 += dpp<ROR8>(o1);
    o0 = swap16_add(o0, o0); o1 = swap16_add(o1, o1);
    o0 = swap32_add(o0, o0); o1 = swap32_add(o1, o1);
    const float inv = __builtin_amdgcn_rcpf(L);
    opk = cvtpk(o0 * inv, o1 * inv);
  }
  STAMP(5);
#pragma unroll
  for (int p = 0; p < 8; ++p) {
    const unsigned os = __builtin_amdgcn_readlane(opk, p);
#pragma unroll
    for (int e = 0; e < 8; ++e) po[e] = dot2(wpk[p][e], os, po[e]);
  }
  if (live) {
#pragma unroll
    for (int k = 0; k < 4; ++k) st_sc1(rs, WS_P + ((lane * 256 + b) * 4 + k) * 16, u32x4{asu(po[2 * k]), asu(po[2 * k + 1]), tagA, tagB});
  }
  if (lane == 63) st_sc1(rs, WS_F + b * 16, u32x4{0u, 0u, tagA, tagB});
  STAMP(6);
  if (b >= 56) { STAMP_OUT(0); return; }
  asm volatile("s_waitcnt vmcnt(0)" ::: "memory");
  {
    float s0 = 0.f, s1 = 0.f;
    for (unsigned spin = 0;; ++spin) {
      u32x4 F[4];
#pragma unroll
      for (int k = 0; k < 4; ++k) F[k] = ld_sc1(rs, lane * 16, WS_F + k * 1024);
      bool ok = true;
#pragma unroll
      for (int i = 0; i < 4; ++i) ok = ok && (F[i][2] == tagA) && (F[i][3] == tagB);
      if (__all(ok) || spin > SPIN_CAP) break;
      __builtin_amdgcn_s_sleep(POLL_SLEEP);
    }
    for (unsigned spin = 0;; ++spin) {
      u32x4 P[16];
#pragma unroll
      for (int k = 0; k < 16; ++k) P[k] = ld_sc1(rs, lane * 16, WS_P + (b * 1024 + 64 * k) * 16);
      bool ok = true;
#pragma unroll
      for (int i = 0; i < 16; ++i) ok = ok && (P[i][2] == tagA) && (P[i][3] == tagB);
      if (__all(ok) || spin > SPIN_CAP) {
#pragma unroll
        for (int k = 0; k < 16; ++k) { s0 += asf(P[k][0]); s1 += asf(P[k][1]); }
        break;
      }
      __builtin_amdgcn_s_sleep(POLL_SLEEP);
    }
    s0 += dpp<ROR4>(s0); s1 += dpp<ROR4>(s1);
    s0 += dpp<ROR8>(s0); s1 += dpp<ROR8>(s1);
    s0 = swap16_add(s0, s0); s1 = swap16_add(s1, s1);
    s0 = swap32_add(s0, s0); s1 = swap32_add(s1, s1);
    const float b2f = blo(b2r);
    const unsigned res = cvtpk(blo(xo) + s0 + b2f, bhi(xo) + s1 + b2f);
    if (lane < 4) *reinterpret_cast<unsigned*>(a.out + 8 * b + 2 * lane) = res;
    if (b == 0 && lane == 0) st_sc1(rs, WS_EPOCH, u32x4{tagA, tagB, 0u, 0u});
  }
  STAMP(7);
  STAMP_OUT(0);
}

extern "C" void kernel_launch(void* const* d_in, const int* in_sizes, int n_in,
                              void* d_out, int out_size, void* d_ws, size_t ws_size,
                              hipStream_t stream) {
  Args a;
  a.x = (const __hip_bfloat16*)d_in[0];
  a.kc = (const __hip_bfloat16*)d_in[1];
  a.vc = (const __hip_bfloat16*)d_in[2];
  a.wq = (const __hip_bfloat16*)d_in[3];
  a.wo = (const __hip_bfloat16*)d_in[4];
  a.w1 = (const __hip_bfloat16*)d_in[6];
  a.b1 = (const __hip_bfloat16*)d_in[7];
  a.w2 = (const __hip_bfloat16*)d_in[8];
  a.b2 = (const __hip_bfloat16*)d_in[9];
  a.out = (__hip_bfloat16*)d_out;
  a.ws = (unsigned char*)d_ws;
  fused_decode<<<256, 576, 0, stream>>>(a);
}
```

```cpp
#include <hip/hip_runtime.h>
#include <hip/hip_bf16.h>
#include <stdint.h>

typedef unsigned u32x4 __attribute__((ext_vector_type(4)));
typedef float f32x4 __attribute__((ext_vector_type(4)));
typedef float f32x2 __attribute__((ext_vector_type(2)));
typedef __bf16 bf16x2_t __attribute__((ext_vector_type(2)));

constexpr int D = 448, SK = 32768, WIN = 4096;
constexpr int WS_EPOCH = 0;
constexpr int WS_Q = 4096;
constexpr int WS_ML = WS_Q + 8 * 4 * 256 * 16;
constexpr int WS_O = WS_ML + 16 * 32 * 16;
constexpr int WS_P = WS_O + 16 * 16 * 256 * 16;
constexpr int WS_F = WS_P + 56 * 256 * 64;
constexpr int WS_END = WS_F + 256 * 16;
constexpr unsigned SPIN_CAP = 20000;
#define LATEPOLL 1
#define POLL_SLEEP 4
#define KFIRST 3
#define VHEAD 4
#define VLEFT 4

struct Args {
  const __hip_bfloat16* x; const __hip_bfloat16* kc; const __hip_bfloat16* vc; const __hip_bfloat16* wq;
  const __hip_bfloat16* wo; const __hip_bfloat16* w1; const __hip_bfloat16* b1; const __hip_bfloat16* w2;
  const __hip_bfloat16* b2; __hip_bfloat16* out; unsigned char* ws;
};

__device__ __forceinline__ float asf(unsigned u) { return __builtin_bit_cast(float, u); }
__device__ __forceinline__ unsigned asu(float f) { return __builtin_bit_cast(unsigned, f); }
__device__ __forceinline__ float blo(unsigned u) { return asf(u << 16); }
__device__ __forceinline__ float bhi(unsigned u) { return asf(u & 0xffff0000u); }
__device__ __forceinline__ unsigned cvtpk(float lo, float hi) {
  bf16x2_t v; v[0] = (__bf16)lo; v[1] = (__bf16)hi; return __builtin_bit_cast(unsigned, v);
}
__device__ __forceinline__ float bfr(float x) { return blo(cvtpk(x, x)); }
__device__ __forceinline__ float dot2(unsigned a, unsigned b, float acc) {
  return __builtin_amdgcn_fdot2_f32_bf16(__builtin_bit_cast(bf16x2_t, a), __builtin_bit_cast(bf16x2_t, b), acc, false);
}
template <int CTRL> __device__ __forceinline__ float dpp(float x) {
  return __builtin_bit_cast(float, __builtin_amdgcn_mov_dpp(__builtin_bit_cast(int, x), CTRL, 0xf, 0xf, true));
}
constexpr int XOR1 = 0xB1, XOR2 = 0x4E, HMIR = 0x141, RMIR = 0x140, ROR8 = 0x128, ROR4 = 0x124;
__device__ __forceinline__ float swap16_add(float a, float b) {
  auto s = __builtin_amdgcn_permlane16_swap(asu(a), asu(b), false, false);
  return asf(s[0]) + asf(s[1]);
}
__device__ __forceinline__ float swap32_add(float a, float b) {
  auto s = __builtin_amdgcn_permlane32_swap(asu(a), asu(b), false, false);
  return asf(s[0]) + asf(s[1]);
}
__device__ __forceinline__ float swap16_max(float a) {
  auto s = __builtin_amdgcn_permlane16_swap(asu(a), asu(a), false, false);
  return fmaxf(asf(s[0]), asf(s[1]));
}
__device__ __forceinline__ float swap32_max(float a) {
  auto s = __builtin_amdgcn_permlane32_swap(asu(a), asu(a), false, false);
  return fmaxf(asf(s[0]), asf(s[1]));
}
__device__ __forceinline__ float wave_sum(float v) {
  v += dpp<XOR1>(v); v += dpp<XOR2>(v); v += dpp<HMIR>(v); v += dpp<RMIR>(v);
  v = swap16_add(v, v); v = swap32_add(v, v); return v;
}
__device__ __forceinline__ float half_max(float v) {
  v = fmaxf(v, dpp<XOR1>(v)); v = fmaxf(v, dpp<XOR2>(v)); v = fmaxf(v, dpp<HMIR>(v)); v = fmaxf(v, dpp<RMIR>(v));
  return swap16_max(v);
}
__device__ __forceinline__ float half_sum(float v) {
  v += dpp<XOR1>(v); v += dpp<XOR2>(v); v += dpp<HMIR>(v); v += dpp<RMIR>(v);
  return swap16_add(v, v);
}
__device__ __forceinline__ u32x4 ld_sc1(__amdgpu_buffer_rsrc_t r, int off, int soff = 0) { return __builtin_amdgcn_raw_buffer_load_b128(r, off, soff, 16); }
typedef __attribute__((address_space(3))) unsigned lds_u32;
__device__ __forceinline__ unsigned lds_flag_load(float* p) { return __hip_atomic_load((lds_u32*)p, __ATOMIC_RELAXED, __HIP_MEMORY_SCOPE_WORKGROUP); }
__device__ __forceinline__ void lds_flag_store(float* p, unsigned v) { __hip_atomic_store((lds_u32*)p, v, __ATOMIC_RELAXED, __HIP_MEMORY_SCOPE_WORKGROUP); }
__device__ __forceinline__ void st_sc1(__amdgpu_buffer_rsrc_t r, int off, u32x4 v) { __builtin_amdgcn_raw_buffer_store_b128(v, r, off, 0, 16); }
__device__ __forceinline__ u32x4 ldnt(const void* p) { return __builtin_nontemporal_load(reinterpret_cast<const u32x4*>(p)); }
#define STAMP(i) do {} while (0)
#define STAMP_DECL
#define STAMP_OUT(base) do {} while (0)
#define WG_BARRIER() asm volatile("s_waitcnt lgkmcnt(0)\n\ts_barrier" ::: "memory")
#define SCHED_FENCE() __builtin_amdgcn_sched_barrier(0)

constexpr int L_XS = 0;
constexpr int L_Q = 448;
constexpr int L_TAG = 704;
constexpr int L_FLG = 706;
constexpr int L_PRG = 707;
constexpr int L_PUB = 1020;
constexpr int L_MAX = 708;
constexpr int L_L = 724;
constexpr int L_P = 740;
constexpr int L_O = 1024;
constexpr int L_END = L_O + 8 * 512;

__device__ __forceinline__ void layernorm8(const float (&v)[8], bool live, float (&o)[8]) {
  float s = 0.f;
#pragma unroll
  for (int e = 0; e < 8; ++e) s += v[e];
  s = live ? s : 0.f;
  const float mean = wave_sum(s) * (1.f / 448.f);
  float q = 0.f;
#pragma unroll
  for (int e = 0; e < 8; ++e) { const float d = v[e] - mean; q = fmaf(d, d, q); }
  q = live ? q : 0.f;
  const float var = bfr(wave_sum(q) * (1.f / 448.f));
  const float mb = bfr(mean);
  const float den = bfr(__builtin_amdgcn_sqrtf(bfr(var + 1e-5f)));
  const float rden = __builtin_amdgcn_rcpf(den);
#pragma unroll
  for (int e = 0; e < 8; ++e) o[e] = bfr(bfr(v[e] - mb) * rden);
}

__global__ void __launch_bounds__(576, 3) fused_decode(Args a) {
  __shared__ __attribute__((aligned(16))) float lds[L_END];
  const int b = blockIdx.x, g = b & 7, s = b >> 3;
  const int lane = threadIdx.x & 63;
  const int wave = __builtin_amdgcn_readfirstlane(threadIdx.x >> 6);
  const __amdgpu_buffer_rsrc_t rs = __builtin_amdgcn_make_buffer_rsrc(a.ws, 0, WS_END, 0x00020000);
  float* const flg = &lds[L_FLG];

  if (wave < 8) {
    const int half = lane >> 5, c = lane & 31;
    STAMP_DECL; STAMP(0);
    const size_t base = ((size_t)g * SK + (SK - WIN) + s * 128 + wave * 16) * 256 + lane * 8;
    u32x4 K[8], V[8];
    WG_BARRIER();
#pragma unroll
    for (int i = 0; i < KFIRST; ++i) K[i] = ldnt(a.kc + base + i * 512);
    SCHED_FENCE();
    while (lds_flag_load(&lds[L_PUB]) == 0u) __builtin_amdgcn_s_sleep(1);
#pragma unroll
    for (int i = KFIRST; i < 8; ++i) K[i] = ldnt(a.kc + base + i * 512);
#pragma unroll
    for (int i = 0; i < VHEAD; ++i) V[i] = ldnt(a.vc + base + i * 512);
    SCHED_FENCE();
    asm volatile("s_waitcnt vmcnt(%0)" :: "n"(VLEFT) : "memory");
    if (lane == 0) __hip_atomic_fetch_add((lds_u32*)&lds[L_PRG], 1u, __ATOMIC_RELAXED, __HIP_MEMORY_SCOPE_WORKGROUP);
    while (lds_flag_load(flg) < 1u) __builtin_amdgcn_s_sleep(1);
#pragma unroll
    for (int i = VHEAD; i < 8; ++i) V[i] = ldnt(a.vc + base + i * 512);
    SCHED_FENCE();
    STAMP(1);
    while (lds_flag_load(flg) < 3u) __builtin_amdgcn_s_sleep(1);
    asm volatile("" ::: "memory");
    STAMP(2);
    const u32x4 q0 = *reinterpret_cast<const u32x4*>(&lds[L_Q + 4 * c]);
    const u32x4 q1 = *reinterpret_cast<const u32x4*>(&lds[L_Q + 128 + 4 * c]);
    const unsigned tagA = asu(lds[L_TAG]), tagB = asu(lds[L_TAG + 1]);
    float x[16];
#pragma unroll
    for (int i = 0; i < 8; ++i) {
      float d0 = 0.f, d1 = 0.f;
#pragma unroll
      for (int k = 0; k < 4; ++k) { d0 = dot2(K[i][k], q0[k], d0); d1 = dot2(K[i][k], q1[k], d1); }
      x[2 * i] = d0; x[2 * i + 1] = d1;
    }
#define TR_STEP(N, CTRL, BIT) _Pragma("unroll") for (int h = 0; h < (N); ++h) { \
      const float keep_ = (c & (BIT)) ? x[h + (N)] : x[h], send_ = (c & (BIT)) ? x[h] : x[h + (N)]; \
      x[h] = keep_ + dpp<CTRL>(send_); }
    TR_STEP(8, ROR8, 8) TR_STEP(4, HMIR, 4) TR_STEP(2, XOR2, 2) TR_STEP(1, XOR1, 1)
#undef TR_STEP
    float sc = swap16_add(x[0], x[0]);
    sc = bfr(sc) * (0.0625f * 1.4426950408889634f);
    float wm = sc;
    wm = fmaxf(wm, dpp<ROR4>(wm)); wm = fmaxf(wm, dpp<ROR8>(wm)); wm = fmaxf(wm, dpp<XOR2>(wm)); wm = swap32_max(wm);
    if (lane < 2) lds[L_MAX + wave * 2 + lane] = wm;
    STAMP(3);
    WG_BARRIER();
    STAMP(4);
    float M = lds[L_MAX + (c & 1)];
#pragma unroll
    for (int w = 1; w < 8; ++w) M = fmaxf(M, lds[L_MAX + w * 2 + (c & 1)]);
    const float p = __builtin_amdgcn_exp2f(sc - M);
    float ls = p;
    ls += dpp<ROR4>(ls); ls += dpp<ROR8>(ls); ls += dpp<XOR2>(ls); ls = swap32_add(ls, ls);
    if (lane < 2) lds[L_L + wave * 2 + lane] = ls;
    float* prow = &lds[L_P + (wave * 2 + half) * 16];
    if (c < 16) prow[c] = p;
    f32x4 pr[4];
#pragma unroll
    for (int u = 0; u < 4; ++u) pr[u] = *reinterpret_cast<const f32x4*>(prow + 4 * u);
    float acc[16];
#pragma unroll
    for (int k = 0; k < 16; ++k) acc[k] = 0.f;
#pragma unroll
    for (int i = 0; i < 8; ++i) {
      const float p0 = pr[i >> 1][(2 * i) & 3], p1 = pr[i >> 1][(2 * i + 1) & 3];
#pragma unroll
      for (int k = 0; k < 4; ++k) {
        const float lo = blo(V[i][k]), hi = bhi(V[i][k]);
        acc[2 * k] = fmaf(p0, lo, acc[2 * k]); acc[2 * k + 1] = fmaf(p0, hi, acc[2 * k + 1]);
        acc[8 + 2 * k] = fmaf(p1, lo, acc[8 + 2 * k]); acc[8 + 2 * k + 1] = fmaf(p1, hi, acc[8 + 2 * k + 1]);
      }
    }
    float r[8];
#pragma unroll
    for (int k = 0; k < 8; ++k) r[k] = swap32_add(acc[k], acc[8 + k]);
    float* orow = &lds[L_O + wave * 512 + half * 256 + 8 * c];
    *reinterpret_cast<f32x4*>(orow) = f32x4{r[0], r[1], r[2], r[3]};
    *reinterpret_cast<f32x4*>(orow + 4) = f32x4{r[4], r[5], r[6], r[7]};
    STAMP(5);
    WG_BARRIER();
    STAMP(6);
    if (wave < 4) {
      const int t = wave * 64 + lane, hd = t >> 7, pi = t & 127;
      f32x2 o = *reinterpret_cast<const f32x2*>(&lds[L_O + 2 * t]);
#pragma unroll
      for (int w = 1; w < 8; ++w) { const f32x2 u = *reinterpret_cast<const f32x2*>(&lds[L_O + w * 512 + 2 * t]); o[0] += u[0]; o[1] += u[1]; }
      st_sc1(rs, WS_O + ((((2 * g + hd) * 16 + (pi >> 3)) * 32 + s) * 8 + (pi & 7)) * 16, u32x4{asu(o[0]), asu(o[1]), tagA, tagB});
      if (wave == 0 && lane < 2) {
        float L = 0.f;
#pragma unroll
        for (int w = 0; w < 8; ++w) L += lds[L_L + w * 2 + lane];
        st_sc1(rs, WS_ML + ((2 * g + lane) * 32 + s) * 16, u32x4{asu(M), asu(L), tagA, tagB});
      }
      asm volatile("s_waitcnt vmcnt(0)" ::: "memory");
      if (lane == 0) __hip_atomic_fetch_add((lds_u32*)&lds[L_PRG], 1u, __ATOMIC_RELAXED, __HIP_MEMORY_SCOPE_WORKGROUP);
    }
    STAMP(7);
    if (wave == 0) STAMP_OUT(8);
    return;
  }

  STAMP_DECL; STAMP(0);
  const bool live = lane < 56;
  const int lc = live ? lane : 0;
  const u32x4 ep = ld_sc1(rs, WS_EPOCH);
  const u32x4 xraw = *reinterpret_cast<const u32x4*>(a.x + 8 * lc);
  const int kq = s >> 3, cb = s & 7;
  u32x4 wqv[14];
  {
    const __hip_bfloat16* wp = a.wq + (size_t)(kq * 112 + (lane >> 3)) * 4096 + g * 512 + cb * 64 + 8 * (lane & 7);
#pragma unroll
    for (int i = 0; i < 14; ++i) wqv[i] = ldnt(wp + (size_t)i * 8 * 4096);
  }
  const int fj = 7 * b + ((lane & 7) < 7 ? (lane & 7) : 6);
  const unsigned w1r = reinterpret_cast<const unsigned short*>(a.w1)[fj];
  const unsigned b1r = reinterpret_cast<const unsigned short*>(a.b1)[fj];
  const unsigned w2r = reinterpret_cast<const unsigned short*>(a.w2)[fj];
  const unsigned tch0 = *reinterpret_cast<const unsigned*>(a.out + 8 * (b % 56) + 2 * (lane & 3));
  const unsigned tch1 = __builtin_amdgcn_raw_buffer_load_b32(rs, lane * 32768, WS_Q, 0);
  const unsigned tch2 = *reinterpret_cast<const unsigned*>(a.wo + (size_t)(2 * g * 256 + s * 16) * D);
  if (lane == 0) { lds_flag_store(flg, 0u); lds_flag_store(&lds[L_PRG], 0u); lds_flag_store(&lds[L_PUB], 0u); }
  SCHED_FENCE();
  WG_BARRIER();
  SCHED_FENCE();
  float xv[8], xn[8], hh[8];
#pragma unroll
  for (int k = 0; k < 4; ++k) { xv[2 * k] = blo(xraw[k]); xv[2 * k + 1] = bhi(xraw[k]); }
  layernorm8(xv, live, xn);
  if (live) {
    *reinterpret_cast<f32x4*>(&lds[L_XS + 8 * lane]) = f32x4{xn[0], xn[1], xn[2], xn[3]};
    *reinterpret_cast<f32x4*>(&lds[L_XS + 8 * lane + 4]) = f32x4{xn[4], xn[5], xn[6], xn[7]};
  }
  const unsigned tagA = ep[0] + 1u, tagB = ep[1] + 0x9E3779B9u;
  STAMP(1);
  {
    float qa[8];
#pragma unroll
    for (int e = 0; e < 8; ++e) qa[e] = 0.f;
#pragma unroll
    for (int i = 0; i < 14; ++i) {
      const float xs = lds[L_XS + kq * 112 + 8 * i + (lane >> 3)];
#pragma unroll
      for (int k = 0; k < 4; ++k) { qa[2 * k] = fmaf(xs, blo(wqv[i][k]), qa[2 * k]); qa[2 * k + 1] = fmaf(xs, bhi(wqv[i][k]), qa[2 * k + 1]); }
    }
#pragma unroll
    for (int h = 0; h < 4; ++h) {
      const float keep = (lane & 8) ? qa[h + 4] : qa[h], send = (lane & 8) ? qa[h] : qa[h + 4];
      qa[h] = keep + dpp<ROR8>(send);
    }
    qa[0] = swap16_add(qa[0], qa[2]); qa[1] = swap16_add(qa[1], qa[3]);
    qa[0] = swap32_add(qa[0], qa[0]); qa[1] = swap32_add(qa[1], qa[1]);
    if (lane < 32) {
      const int pidx = cb * 32 + 4 * (lane & 7) + 2 * ((lane >> 3) & 1) + ((lane >> 4) & 1);
      st_sc1(rs, WS_Q + ((g * 4 + kq) * 256 + pidx) * 16, u32x4{asu(qa[0]), asu(qa[1]), tagA, tagB});
    }
  }
  SCHED_FENCE();
  if (lane == 0) lds_flag_store(&lds[L_PUB], 1u);
  STAMP(2);
  SCHED_FENCE();
  layernorm8(xn, live, hh);
  float po[8];
  {
#pragma unroll
    for (int e = 0; e < 8; ++e) po[e] = 0.f;
#pragma unroll 1
    for (int j = 0; j < 7; ++j) {
      const float w1f = blo(__builtin_amdgcn_readlane(w1r, j)), b1f = blo(__builtin_amdgcn_readlane(b1r, j)), w2f = blo(__builtin_amdgcn_readlane(w2r, j));
#pragma unroll
      for (int e = 0; e < 8; ++e) {
        const float u = fmaf(hh[e], w1f, b1f);
        const float z = u * (1.f + 0.044715f * u * u) * (-2.f * 0.7978845608028654f * 1.4426950408889634f);
        const float gl = u * __builtin_amdgcn_rcpf(1.f + __builtin_amdgcn_exp2f(z));
        po[e] = fmaf(gl, w2f, po[e]);
      }
    }
#pragma unroll
    for (int e = 0; e < 8; ++e) asm volatile("" : "+v"(po[e]));
  }
  SCHED_FENCE();
  while (lds_flag_load(&lds[L_PRG]) < 8u) __builtin_amdgcn_s_sleep(1);
  for (unsigned spin = 0;; ++spin) {
    u32x4 v[16];
#pragma unroll
    for (int j = 0; j < 4; ++j)
#pragma unroll
      for (int k = 0; k < 4; ++k) v[j * 4 + k] = ld_sc1(rs, lane * 16, WS_Q + ((g * 4 + k) * 256 + 64 * j) * 16);
    SCHED_FENCE();
    if (lane == 0) lds_flag_store(flg, 1u);
    SCHED_FENCE();
    bool ok = true;
#pragma unroll
    for (int i = 0; i < 16; ++i) ok = ok && (v[i][2] == tagA) && (v[i][3] == tagB);
    if (__all(ok) || spin > SPIN_CAP) {
#pragma unroll
      for (int j = 0; j < 4; ++j) {
        const float s0 = (asf(v[j * 4][0]) + asf(v[j * 4 + 1][0])) + (asf(v[j * 4 + 2][0]) + asf(v[j * 4 + 3][0]));
        const float s1 = (asf(v[j * 4][1]) + asf(v[j * 4 + 1][1])) + (asf(v[j * 4 + 2][1]) + asf(v[j * 4 + 3][1]));
        lds[L_Q + lane + 64 * j] = asf(cvtpk(s0, s1));
      }
      break;
    }
  }
  asm volatile("" :: "v"(tch0), "v"(tch1), "v"(tch2));
  STAMP(3);
  if (lane == 0) { lds[L_TAG] = asf(tagA); lds[L_TAG + 1] = asf(tagB); }
  asm volatile("s_waitcnt lgkmcnt(0)" ::: "memory");
  if (lane == 0) lds_flag_store(flg, 3u);
  SCHED_FENCE();
  const int h = 2 * g + (s >> 4), db = s & 15;
  u32x4 wov[16];
  {
    const __hip_bfloat16* wp = a.wo + (size_t)(h * 256 + db * 16) * D + 8 * lc;
#pragma unroll
    for (int r = 0; r < 16; ++r) wov[r] = ldnt(wp + r * D);
  }
  unsigned xo = 0, b2r = 0;
  if (b < 56) { xo = *reinterpret_cast<const unsigned*>(a.x + 8 * b + 2 * (lane & 3)); b2r = reinterpret_cast<const unsigned short*>(a.b2)[0]; }
  SCHED_FENCE();
  WG_BARRIER();
  unsigned wpk[8][8];
#pragma unroll
  for (int p = 0; p < 8; ++p)
#pragma unroll
    for (int k = 0; k < 4; ++k) {
      wpk[p][2 * k] = __builtin_amdgcn_perm(wov[2 * p + 1][k], wov[2 * p][k], 0x05040100u);
      wpk[p][2 * k + 1] = __builtin_amdgcn_perm(wov[2 * p + 1][k], wov[2 * p][k], 0x07060302u);
    }
#pragma unroll
  for (int p = 0; p < 8; ++p)
#pragma unroll
    for (int e = 0; e < 8; ++e) asm volatile("" : "+v"(wpk[p][e]));
  SCHED_FENCE();
  WG_BARRIER();
  while (lds_flag_load(&lds[L_PRG]) < 12u) __builtin_amdgcn_s_sleep(1);
  STAMP(4);
  unsigned opk = 0;
  {
    u32x4 R[5];
    for (unsigned spin = 0;; ++spin) {
      R[4] = ld_sc1(rs, (lane & 31) * 16, WS_ML + h * 32 * 16);
      if (__all((R[4][2] == tagA) && (R[4][3] == tagB)) || spin > SPIN_CAP) break;
      __builtin_amdgcn_s_sleep(POLL_SLEEP);
    }
    for (unsigned spin = 0;; ++spin) {
#pragma unroll
      for (int k = 0; k < 4; ++k) R[k] = ld_sc1(rs, lane * 16, WS_O + ((h * 16 + db) * 256 + 64 * k) * 16);
      bool ok = true;
#pragma unroll
      for (int i = 0; i < 4; ++i) ok = ok && (R[i][2] == tagA) && (R[i][3] == tagB);
      if (__all(ok) || spin > SPIN_CAP) break;
      __builtin_amdgcn_s_sleep(POLL_SLEEP);
    }
    const float m = asf(R[4][0]), l = asf(R[4][1]);
    const float M = half_max(m);
    const float w = __builtin_amdgcn_exp2f(m - M);
    const float L = half_sum(w * l);
    float o0 = 0.f, o1 = 0.f;
#pragma unroll
    for (int k = 0; k < 4; ++k) {
      const float wk = __shfl(w, (lane >> 3) + 8 * k);
      o0 = fmaf(wk, asf(R[k][0]), o0); o1 = fmaf(wk, asf(R[k][1]), o1);
    }
    o0 += dpp<ROR8>(o0); o1 += dpp<ROR8>(o1);
    o0 = swap16_add(o0, o0); o1 = swap16_add(o1, o1);
    o0 = swap32_add(o0, o0); o1 = swap32_add(o1, o1);
    const float inv = __builtin_amdgcn_rcpf(L);
    opk = cvtpk(o0 * inv, o1 * inv);
  }
  STAMP(5);
#pragma unroll
  for (int p = 0; p < 8; ++p) {
    const unsigned os = __builtin_amdgcn_readlane(opk, p);
#pragma unroll
    for (int e = 0; e < 8; ++e) po[e] = dot2(wpk[p][e], os, po[e]);
  }
  if (live) {
#pragma unroll
    for (int k = 0; k < 4; ++k) st_sc1(rs, WS_P + ((lane * 256 + b) * 4 + k) * 16, u32x4{asu(po[2 * k]), asu(po[2 * k + 1]), tagA, tagB});
  }
  if (lane == 63) st_sc1(rs, WS_F + b * 16, u32x4{0u, 0u, tagA, tagB});
  STAMP(6);
  if (b >= 56) { STAMP_OUT(0); return; }
  asm volatile("s_waitcnt vmcnt(0)" ::: "memory");
  {
    float s0 = 0.f, s1 = 0.f;
    for (unsigned spin = 0;; ++spin) {
      u32x4 F[4];
#pragma unroll
      for (int k = 0; k < 4; ++k) F[k] = ld_sc1(rs, lane * 16, WS_F + k * 1024);
      bool ok = true;
#pragma unroll
      for (int i = 0; i < 4; ++i) ok = ok && (F[i][2] == tagA) && (F[i][3] == tagB);
      if (__all(ok) || spin > SPIN_CAP) break;
      __builtin_amdgcn_s_sleep(POLL_SLEEP);
    }
    for (unsigned spin = 0;; ++spin) {
      u32x4 P[16];
#pragma unroll
      for (int k = 0; k < 16; ++k) P[k] = ld_sc1(rs, lane * 16, WS_P + (b * 1024 + 64 * k) * 16);
      bool ok = true;
#pragma unroll
      for (int i = 0; i < 16; ++i) ok = ok && (P[i][2] == tagA) && (P[i][3] == tagB);
      if (__all(ok) || spin > SPIN_CAP) {
#pragma unroll
        for (int k = 0; k < 16; ++k) { s0 += asf(P[k][0]); s1 += asf(P[k][1]); }
        break;
      }
      __builtin_amdgcn_s_sleep(POLL_SLEEP);
    }
    s0 += dpp<ROR4>(s0); s1 += dpp<ROR4>(s1);
    s0 += dpp<ROR8>(s0); s1 += dpp<ROR8>(s1);
    s0 = swap16_add(s0, s0); s1 = swap16_add(s1, s1);
    s0 = swap32_add(s0, s0); s1 = swap32_add(s1, s1);
    const float b2f = blo(b2r);
    const unsigned res = cvtpk(blo(xo) + s0 + b2f, bhi(xo) + s1 + b2f);
    if (lane < 4) *reinterpret_cast<unsigned*>(a.out + 8 * b + 2 * lane) = res;
    if (b == 0 && lane == 0) st_sc1(rs, WS_EPOCH, u32x4{tagA, tagB, 0u, 0u});
  }
  STAMP(7);
  STAMP_OUT(0);
}

extern "C" void kernel_launch(void* const* d_in, const int* in_sizes, int n_in,
                              void* d_out, int out_size, void* d_ws, size_t ws_size,
                              hipStream_t stream) {
  Args a;
  a.x = (const __hip_bfloat16*)d_in[0];
  a.kc = (const __hip_bfloat16*)d_in[1];
  a.vc = (const __hip_bfloat16*)d_in[2];
  a.wq = (const __hip_bfloat16*)d_in[3];
  a.wo = (const __hip_bfloat16*)d_in[4];
  a.w1 = (const __hip_bfloat16*)d_in[6];
  a.b1 = (const __hip_bfloat16*)d_in[7];
  a.w2 = (const __hip_bfloat16*)d_in[8];
  a.b2 = (const __hip_bfloat16*)d_in[9];
  a.out = (__hip_bfloat16*)d_out;
  a.ws = (unsigned char*)d_ws;
  fused_decode<<<256, 576, 0, stream>>>(a);
}
```
